```python
import torch

def _softmax(x, axis=-1):
    return torch.softmax(x, dim=axis)
def _gelu(x):
    return torch.nn.functional.gelu(x, approximate="tanh")
def _silu(x):
    return torch.nn.functional.silu(x)
def _sigmoid(x):
    return torch.sigmoid(x)
def _layernorm(x, axis=-1, eps=1e-5):
    m = x.mean(dim=axis, keepdim=True)
    v = x.var(dim=axis, keepdim=True, unbiased=False)
    return (x - m) / torch.sqrt(v + eps)
def _scatter_add(tgt, idx, src, axis=0):
    return tgt.clone().index_add_(axis, idx, src)
def _segment_sum(src, seg, n):
    out = torch.zeros((n,) + src.shape[1:], dtype=src.dtype)
    return out.index_add_(0, seg, src)
def _pad(x, pw):
    flat = tuple(v for pair in reversed(pw) for v in pair)
    return torch.nn.functional.pad(x, flat)

kernel_name = 'parallel_sum__gqa_block_cached__dense_activation_squared_relu__decode'

def setup_inputs():

    _w = (lambda a: (lambda t: t.to(torch.bfloat16) if t.is_floating_point() else t)(torch.as_tensor(a)))
    def _A_setup():
        B, Sq, D = 4, 1, 640
        Hq, Hkv, Sk, Dh = 32, 32, 8192, 192
        assert Hq % Hkv == 0, (Hq, Hkv)
        return {
            "x": _w((lambda *s: torch.randn(*s, dtype=torch.bfloat16) * 0.05)(B, Sq, D)),
            "k_cache": _w((lambda *s: torch.randn(*s, dtype=torch.bfloat16) * 1.6)(B, Hkv, Sk, Dh)),
            "v_cache": _w((lambda *s: torch.randn(*s, dtype=torch.bfloat16) * 3.2)(B, Hkv, Sk, Dh)),
            "wq": _w((lambda *s: torch.randn(*s, dtype=torch.bfloat16) * 1.6)(D, Hq * Dh) * (D ** -0.5)),
            "wo": _w((lambda *s: torch.randn(*s, dtype=torch.bfloat16) * 3.2)(Hq * Dh, D) * ((Hq * Dh) ** -0.5)),
        }
    _A_in = _A_setup()

    def _B_setup():
        B, S, D, F = 4, 1, 640, 1536
        return {
            "x": _w((lambda *s: torch.randn(*s, dtype=torch.bfloat16) * 0.05)(B, S, D)),
            "w1": _w((lambda *s: torch.randn(*s, dtype=torch.bfloat16) * 0.05)(D, F)),
            "b1": _w((lambda *s: torch.randn(*s, dtype=torch.bfloat16) * 0.2)(F)),
            "w2": _w((lambda *s: torch.randn(*s, dtype=torch.bfloat16) * 0.05)(F, D)),
            "b2": _w((lambda *s: torch.randn(*s, dtype=torch.bfloat16) * 0.8)(D)),
        }
    _B_in = _B_setup()

    return {
        "A_x": _A_in["x"],
        "A_k_cache": _A_in["k_cache"],
        "A_v_cache": _A_in["v_cache"],
        "A_wq": _A_in["wq"],
        "A_wo": _A_in["wo"],
        "B_x": _B_in["x"],
        "B_w1": _B_in["w1"],
        "B_b1": _B_in["b1"],
        "B_w2": _B_in["w2"],
        "B_b2": _B_in["b2"],
    }

def reference(A_x, A_k_cache, A_v_cache, A_wq, A_wo, B_x, B_w1, B_b1, B_w2, B_b2):
    def _A_ref(x, k_cache, v_cache, wq, wo):
        B, Sq, D = x.shape
        Hq, Dh = 32, 192
        Hkv = k_cache.shape[1]
        rep = Hq // Hkv
        q = (lambda x, *d: x.permute(*d))((x @ wq).reshape(B, Sq, Hq, Dh), 0, 2, 1, 3)
        k = (lambda x, n, axis: torch.repeat_interleave(x, n, dim=axis))(k_cache, rep, axis=1)
        v = (lambda x, n, axis: torch.repeat_interleave(x, n, dim=axis))(v_cache, rep, axis=1)
        scores = torch.einsum("bhqd,bhkd->bhqk", q, k) * (Dh ** -0.5)
        p = _softmax(scores, axis=-1)
        o = torch.einsum("bhqk,bhkd->bhqd", p, v)
        return (lambda x, *d: x.permute(*d))(o, 0, 2, 1, 3).reshape(B, Sq, Hq * Dh) @ wo

    def _B_ref(x, w1, b1, w2, b2):
        h = torch.einsum("bsd,df->bsf", x, w1) + b1
        zero = torch.zeros_like(h)
        r = torch.where(h > zero, h, zero)
        h = r * r
        out = torch.einsum("bsf,fd->bsd", h, w2) + b2
        return out

    x = A_x
    xn = _layernorm(x, axis=-1)
    return x + _A_ref(xn, A_k_cache, A_v_cache, A_wq, A_wo) + _B_ref(xn, B_w1, B_b1, B_w2, B_b2)
```

```cpp
#include <hip/hip_runtime.h>
#include <hip/hip_bf16.h>
#include <stdint.h>

typedef unsigned u32x4 __attribute__((ext_vector_type(4)));
typedef float f32x4 __attribute__((ext_vector_type(4)));
typedef __bf16 bf16x2 __attribute__((ext_vector_type(2)));
typedef __amdgpu_buffer_rsrc_t rsrc_t;
#define LAS __attribute__((address_space(3)))

constexpr int NB = 4, DM = 640, NH = 32, SK = 8192, DH = 192, FF = 1536;
constexpr int NWG = 256, NTHR = 512;
constexpr int TILE_B = 8 * DH * 2;
constexpr int TILES = SK / 2 / 8;
constexpr int TPW = TILES / 4;
#define RING 8
#define ROTMUL 37
#define WQWIN 10
constexpr int R = RING;
constexpr float LOG2E = 1.4426950408889634f;
constexpr float SCALE = 0.07216878364870322f;
constexpr unsigned OOB = 0x40000000u;

constexpr size_t WS_Y = 0, WS_ML = (size_t)NB * 40 * 128 * 16 * 4;

constexpr int L_WO = 0;
constexpr int L_YP = L_WO + 96 * 1280;
constexpr int L_OW = L_YP + 6 * 640 * 4;
constexpr int L_OP = L_OW + 4 * 192 * 4;
constexpr int L_XN = L_OP + 192 * 4;
constexpr int L_QP = L_XN + 640 * 4;
constexpr int L_HP = L_QP + 4 * 192 * 4;
constexpr int L_ML = L_HP + 4 * 32 * 4;
constexpr int L_END = L_ML + 64;

__device__ __forceinline__ rsrc_t mkrs(const void* p, unsigned bytes) {
  return __builtin_amdgcn_make_buffer_rsrc((void*)p, 0, bytes, 0x00020000);
}
template <int AUX> __device__ __forceinline__ u32x4 bld(rsrc_t r, unsigned voff, unsigned soff) {
  return __builtin_amdgcn_raw_buffer_load_b128(r, voff, soff, AUX);
}
__device__ __forceinline__ float dot2(unsigned a, unsigned b, float c) {
  return __builtin_amdgcn_fdot2_f32_bf16(__builtin_bit_cast(bf16x2, a), __builtin_bit_cast(bf16x2, b), c, false);
}
__device__ __forceinline__ float bflo(unsigned v) { return __builtin_bit_cast(float, v << 16); }
__device__ __forceinline__ float bfhi(unsigned v) { return __builtin_bit_cast(float, v & 0xffff0000u); }
__device__ __forceinline__ float rbf(float x) { return (float)(__bf16)x; }
__device__ __forceinline__ unsigned pkbf(float lo, float hi) {
  bf16x2 v = {(__bf16)lo, (__bf16)hi};
  return __builtin_bit_cast(unsigned, v);
}
template <int CTRL> __device__ __forceinline__ float dpp(float x) {
  return __builtin_bit_cast(float, __builtin_amdgcn_mov_dpp(__builtin_bit_cast(int, x), CTRL, 0xf, 0xf, true));
}
constexpr int XOR1 = 0xB1, XOR2 = 0x4E, XOR7 = 0x141, XOR8 = 0x128;
__device__ __forceinline__ float rs32(float a, float b) {
  auto s = __builtin_amdgcn_permlane32_swap(__float_as_uint(a), __float_as_uint(b), false, false);
  return __uint_as_float(s[0]) + __uint_as_float(s[1]);
}
__device__ __forceinline__ float rs16(float a, float b) {
  auto s = __builtin_amdgcn_permlane16_swap(__float_as_uint(a), __float_as_uint(b), false, false);
  return __uint_as_float(s[0]) + __uint_as_float(s[1]);
}
__device__ __forceinline__ float rs8(float a, float b, bool up) {
  const float t1 = a + dpp<XOR8>(a), t2 = b + dpp<XOR8>(b);
  return up ? t2 : t1;
}
__device__ __forceinline__ float max_x16_32(float x) {
  auto s = __builtin_amdgcn_permlane16_swap(__float_as_uint(x), __float_as_uint(x), false, false);
  x = fmaxf(__uint_as_float(s[0]), __uint_as_float(s[1]));
  auto t = __builtin_amdgcn_permlane32_swap(__float_as_uint(x), __float_as_uint(x), false, false);
  return fmaxf(__uint_as_float(t[0]), __uint_as_float(t[1]));
}
__device__ __forceinline__ float wave_sum(float x) {
  x += dpp<XOR1>(x); x += dpp<XOR2>(x); x += dpp<XOR7>(x); x += dpp<XOR8>(x);
  x = rs16(x, x);
  return rs32(x, x);
}
__device__ __forceinline__ void reduce_scatter24(const float (&a)[24], float (&r)[3], int lane) {
#pragma unroll
  for (int i = 0; i < 3; ++i) {
    float u[4], v[2];
#pragma unroll
    for (int k = 0; k < 4; ++k) u[k] = rs32(a[i * 8 + k], a[i * 8 + k + 4]);
#pragma unroll
    for (int k = 0; k < 2; ++k) v[k] = rs16(u[k], u[k + 2]);
    r[i] = rs8(v[0], v[1], lane & 8);
  }
}
__device__ __forceinline__ void fma8(float* acc, u32x4 w, float s) {
#pragma unroll
  for (int k = 0; k < 4; ++k) {
    acc[2 * k] = fmaf(s, bflo(w[k]), acc[2 * k]);
    acc[2 * k + 1] = fmaf(s, bfhi(w[k]), acc[2 * k + 1]);
  }
}
#define LDS_FENCE() asm volatile("s_waitcnt lgkmcnt(0)" ::: "memory")
#define BARRIER() do { LDS_FENCE(); __builtin_amdgcn_s_barrier(); asm volatile("" ::: "memory"); } while (0)
#define PIN24(a) asm volatile("" : "+v"(a[0]), "+v"(a[1]), "+v"(a[2]), "+v"(a[3]), "+v"(a[4]), "+v"(a[5]), "+v"(a[6]), "+v"(a[7]), \
    "+v"(a[8]), "+v"(a[9]), "+v"(a[10]), "+v"(a[11]), "+v"(a[12]), "+v"(a[13]), "+v"(a[14]), "+v"(a[15]), \
    "+v"(a[16]), "+v"(a[17]), "+v"(a[18]), "+v"(a[19]), "+v"(a[20]), "+v"(a[21]), "+v"(a[22]), "+v"(a[23]) :: "memory")
#define STAMP(i) do {} while (0)

__global__ void __launch_bounds__(NTHR, 2)
attn_main(const unsigned short* __restrict__ x, const unsigned char* __restrict__ kc, const unsigned char* __restrict__ vc,
          const unsigned char* __restrict__ wq, const unsigned char* __restrict__ wo, const unsigned char* __restrict__ w1,
          const unsigned short* __restrict__ b1, const unsigned char* __restrict__ w2, unsigned char* __restrict__ ws) {
  __shared__ __attribute__((aligned(16))) unsigned char lds[L_END];
  const int tid = threadIdx.x, lane = tid & 63, wave = __builtin_amdgcn_readfirstlane(tid >> 6);
  const int bid = blockIdx.x;
  const int h = (bid & 7) * 4 + ((bid >> 3) & 3), half = (bid >> 5) & 1, b = bid >> 6;
  const int src = h * 2 + half;
  const int g = lane >> 3, c = lane & 7;
  float* const YP = (float*)(lds + L_YP);
  float* const OW = (float*)(lds + L_OW);
  float* const OP = (float*)(lds + L_OP);
  float* const XN = (float*)(lds + L_XN);
  float* const QP = (float*)(lds + L_QP);
  float* const HP = (float*)(lds + L_HP);
  float* const ML = (float*)(lds + L_ML);
  float* const Y = (float*)(ws + WS_Y) + (size_t)b * 40 * 128 * 16;
  STAMP(0);
  const int t4 = tid & 255;
  const int ycc = t4 % 80, yrg = t4 / 80;
  float ya[8];
#pragma unroll
  for (int k = 0; k < 8; ++k) ya[k] = 0.f;

  if (wave < 4) {
    const int hw = wave;
    const rsrc_t xr = mkrs(x + b * DM, DM * 2);
    const u32x4 xa = bld<0>(xr, lane * 16, 0);
    const unsigned xb = __builtin_amdgcn_raw_buffer_load_b32(xr, 1024 + lane * 4, 0, 0);
    const rsrc_t wqr = mkrs(wq + h * (DH * 2), DM * NH * DH * 2 - h * (DH * 2));
    const unsigned wqo = (unsigned)(160 * hw + g) * (NH * DH * 2) + c * 16;
    u32x4 wb[WQWIN][3];
#pragma unroll
    for (int j = 0; j < WQWIN; ++j)
#pragma unroll
      for (int i = 0; i < 3; ++i) wb[j][i] = bld<0>(wqr, wqo + i * 128, j * 8 * (NH * DH * 2));
    __builtin_amdgcn_sched_barrier(0);
    float xv[10];
#pragma unroll
    for (int k = 0; k < 4; ++k) { xv[2 * k] = bflo(xa[k]); xv[2 * k + 1] = bfhi(xa[k]); }
    xv[8] = bflo(xb); xv[9] = bfhi(xb);
    float sm = 0.f;
#pragma unroll
    for (int k = 0; k < 10; ++k) sm += xv[k];
    const float mu = wave_sum(sm) * (1.f / DM);
    float sq = 0.f;
#pragma unroll
    for (int k = 0; k < 10; ++k) { const float d = xv[k] - mu; sq = fmaf(d, d, sq); }
    const float var = rbf(wave_sum(sq) * (1.f / DM));
    const float mub = rbf(mu);
    const float sd = rbf(__fsqrt_rn(rbf(var + 1e-5f)));
#pragma unroll
    for (int k = 0; k < 10; ++k) xv[k] = rbf(__fdiv_rn(rbf(xv[k] - mub), sd));
    *(f32x4*)(XN + lane * 8) = f32x4{xv[0], xv[1], xv[2], xv[3]};
    *(f32x4*)(XN + lane * 8 + 4) = f32x4{xv[4], xv[5], xv[6], xv[7]};
    *(float2*)(XN + 512 + lane * 2) = make_float2(xv[8], xv[9]);
    LDS_FENCE();
    float qa[24];
#pragma unroll
    for (int k = 0; k < 24; ++k) qa[k] = 0.f;
#pragma unroll
    for (int j = 0; j < 20; ++j) {
      const float s = XN[160 * hw + 8 * j + g];
#pragma unroll
      for (int i = 0; i < 3; ++i) fma8(qa + 8 * i, wb[j % WQWIN][i], s);
      PIN24(qa);
      __builtin_amdgcn_sched_barrier(0);
      if (j + WQWIN < 20) {
#pragma unroll
        for (int i = 0; i < 3; ++i) wb[j % WQWIN][i] = bld<0>(wqr, wqo + i * 128, (j + WQWIN) * 8 * (NH * DH * 2));
      }
      __builtin_amdgcn_sched_barrier(0);
    }
    {
      float r[3];
      reduce_scatter24(qa, r, lane);
#pragma unroll
      for (int i = 0; i < 3; ++i) QP[hw * 192 + 64 * i + 8 * c + g] = r[i];
    }
    STAMP(1);
    BARRIER();
    const rsrc_t w1r = mkrs(w1, DM * FF * 2);
    const unsigned w1o = (lane & 3) < 3 ? (unsigned)(160 * hw + (lane >> 2)) * (FF * 2) + src * 48 + (lane & 3) * 16 : OOB;
    u32x4 hb[10];
#pragma unroll
    for (int j = 0; j < 10; ++j) hb[j] = bld<0>(w1r, w1o, j * 16 * (FF * 2));
    const rsrc_t w2r = mkrs(w2 + (size_t)src * 24 * (DM * 2), 24 * DM * 2);
    const int cc2 = lane % 20, rg2 = lane / 20;
    const unsigned w2o = lane < 60 ? (unsigned)rg2 * (DM * 2) + (20 * hw + cc2) * 16 : OOB;
    u32x4 gb[8];
#pragma unroll
    for (int j = 0; j < 8; ++j) gb[j] = bld<0>(w2r, w2o, j * 3 * (DM * 2));
    const float b1v = lane < 24 ? bflo(b1[src * 24 + lane]) : 0.f;
    float ha[8];
#pragma unroll
    for (int k = 0; k < 8; ++k) ha[k] = 0.f;
#pragma unroll
    for (int j = 0; j < 10; ++j) fma8(ha, hb[j], XN[160 * hw + 16 * j + (lane >> 2)]);
    {
      float u[4], v[2];
#pragma unroll
      for (int k = 0; k < 4; ++k) u[k] = rs32(ha[k], ha[k + 4]);
#pragma unroll
      for (int k = 0; k < 2; ++k) v[k] = rs16(u[k], u[k + 2]);
      float r = rs8(v[0], v[1], lane & 8);
      r += __shfl_xor(r, 4);
      if ((lane & 4) == 0) HP[hw * 32 + (lane & 3) * 8 + g] = r;
    }
    BARRIER();
    {
      float hv = 0.f;
      if (lane < 24) {
        hv = rbf(rbf((HP[lane] + HP[32 + lane]) + (HP[64 + lane] + HP[96 + lane])) + b1v);
        hv = hv > 0.f ? hv : 0.f;
        hv = rbf(hv * hv);
      }
      float y2[8];
#pragma unroll
      for (int k = 0; k < 8; ++k) y2[k] = 0.f;
#pragma unroll
      for (int j = 0; j < 8; ++j) fma8(y2, gb[j], __shfl(hv, (rg2 + 3 * j) & 63));
#pragma unroll
      for (int k = 0; k < 8; ++k) y2[k] = (y2[k] + __shfl(y2[k], (lane + 20) & 63)) + __shfl(y2[k], (lane + 40) & 63);
      if (lane < 20) {
        const int ccg = 20 * hw + lane;
        float* dst = Y + ((size_t)(ccg >> 1) * 128 + 64 + src) * 16 + (ccg & 1) * 8;
        *(f32x4*)dst = f32x4{y2[0], y2[1], y2[2], y2[3]};
        *(f32x4*)(dst + 4) = f32x4{y2[4], y2[5], y2[6], y2[7]};
      }
    }
    const unsigned char* woh = wo + (size_t)h * DH * (DM * 2);
#pragma unroll
    for (int i = 0; i < 30; ++i)
      __builtin_amdgcn_global_load_lds((const unsigned*)(woh + (hw * 30 + i) * 1024 + lane * 16),
                                       (LAS unsigned*)(lds + L_WO + (hw * 30 + i) * 1024), 16, 0, 0);
    const rsrc_t wor = mkrs(woh + 96 * (DM * 2), 96 * DM * 2);
    const unsigned woo = t4 < 240 ? (unsigned)t4 * 16 : OOB;
    u32x4 wr[32];
#pragma unroll
    for (int j = 0; j < 32; ++j) wr[j] = bld<0>(wor, woo, j * 3840);
    asm volatile("s_waitcnt vmcnt(0)" ::: "memory");
    STAMP(2);
    BARRIER();
    if (tid < 192) {
      const float M = fmaxf(fmaxf(ML[0], ML[2]), fmaxf(ML[4], ML[6]));
      float o = 0.f, L = 0.f;
#pragma unroll
      for (int w = 0; w < 4; ++w) {
        const float e = __builtin_amdgcn_exp2f((ML[2 * w] - M) * LOG2E);
        o = fmaf(e, OW[w * 192 + tid], o);
        L = fmaf(e, ML[2 * w + 1], L);
      }
      const int rr = tid % 96;
      OP[((tid / 96) * 3 + rr % 3) * 32 + rr / 3] = o;
      if (tid == 0) *(float2*)(ws + WS_ML + ((size_t)b * 64 + src) * 8) = make_float2(M, L);
    }
    BARRIER();
    const float* op = OP + (3 + (yrg < 3 ? yrg : 0)) * 32;
#pragma unroll
    for (int j4 = 0; j4 < 8; ++j4) {
      const f32x4 ov = *(const f32x4*)(op + 4 * j4);
#pragma unroll
      for (int u = 0; u < 4; ++u) fma8(ya, wr[4 * j4 + u], ov[u]);
    }
  } else {
    const int sw = wave - 4;
    const size_t base = ((size_t)(b * NH + h) * SK + (size_t)half * (SK / 2)) * (DH * 2);
    const rsrc_t krs = mkrs(kc + base, TILES * TILE_B);
    const rsrc_t vrs = mkrs(vc + base, TILES * TILE_B);
    const unsigned loff = g * (DH * 2) + c * 16;
    const int t0 = sw + (b * 64 + src) * ROTMUL;
    u32x4 kb[R][3], vb[R][3];
#define TOFF(s) ((unsigned)((4 * (s) + t0) & (TILES - 1)) * TILE_B)
#define LOADK(j, o) do { _Pragma("unroll") for (int i_ = 0; i_ < 3; ++i_) kb[j][i_] = bld<2>(krs, loff + i_ * 128, (o)); } while (0)
#define LOADV(j, o) do { _Pragma("unroll") for (int i_ = 0; i_ < 3; ++i_) vb[j][i_] = bld<2>(vrs, loff + i_ * 128, (o)); } while (0)
#pragma unroll
    for (int j = 0; j < R; ++j) { const unsigned o = TOFF(j); LOADK(j, o); LOADV(j, o); __builtin_amdgcn_sched_barrier(0); }
    STAMP(1);
    BARRIER();
    u32x4 q[3];
#pragma unroll
    for (int i = 0; i < 3; ++i) {
      f32x4 s0, s1;
      const float* qp = QP + 64 * i + 8 * c;
      s0 = (*(const f32x4*)(qp) + *(const f32x4*)(qp + 192)) + (*(const f32x4*)(qp + 384) + *(const f32x4*)(qp + 576));
      s1 = (*(const f32x4*)(qp + 4) + *(const f32x4*)(qp + 196)) + (*(const f32x4*)(qp + 388) + *(const f32x4*)(qp + 580));
      q[i] = u32x4{pkbf(s0[0], s0[1]), pkbf(s0[2], s0[3]), pkbf(s1[0], s1[1]), pkbf(s1[2], s1[3])};
    }
    float m = -3.0e38f, l = 0.f, acc[24];
#pragma unroll
    for (int k = 0; k < 24; ++k) acc[k] = 0.f;
    STAMP(2);
#define TILE(j, LK, LV) do { \
      float s0_ = 0.f, s1_ = 0.f, s2_ = 0.f; \
      _Pragma("unroll") for (int k_ = 0; k_ < 4; ++k_) { \
        s0_ = dot2(kb[j][0][k_], q[0][k_], s0_); s1_ = dot2(kb[j][1][k_], q[1][k_], s1_); s2_ = dot2(kb[j][2][k_], q[2][k_], s2_); } \
      float s_ = (s0_ + s1_) + s2_; \
      asm volatile("" : "+v"(s_) :: "memory"); \
      __builtin_amdgcn_sched_barrier(0); \
      LK; \
      __builtin_amdgcn_sched_barrier(0); \
      s_ += dpp<XOR1>(s_); s_ += dpp<XOR2>(s_); s_ += dpp<XOR7>(s_); \
      s_ = rbf(rbf(s_) * SCALE); \
      const float mn_ = fmaxf(m, s_); \
      const float al_ = __builtin_amdgcn_exp2f((m - mn_) * LOG2E), p_ = __builtin_amdgcn_exp2f((s_ - mn_) * LOG2E); \
      m = mn_; l = fmaf(l, al_, p_); \
      _Pragma("unroll") for (int k_ = 0; k_ < 24; ++k_) acc[k_] *= al_; \
      _Pragma("unroll") for (int i_ = 0; i_ < 3; ++i_) fma8(acc + 8 * i_, vb[j][i_], p_); \
      PIN24(acc); \
      __builtin_amdgcn_sched_barrier(0); \
      LV; \
      __builtin_amdgcn_sched_barrier(0); \
    } while (0)
#pragma unroll
    for (int j = 0; j < R; ++j) { const unsigned o = TOFF(R + j); TILE(j, LOADK(j, o), LOADV(j, o)); }
    __builtin_amdgcn_s_barrier();
#pragma nounroll
    for (int t = 2 * R; t < TPW; t += R) {
#pragma unroll
      for (int j = 0; j < R; ++j) { const unsigned o = TOFF(t + j); TILE(j, LOADK(j, o), LOADV(j, o)); }
    }
#pragma unroll
    for (int j = 0; j < R; ++j) TILE(j, (void)0, (void)0);
    STAMP(3);
    const float Mw = max_x16_32(fmaxf(m, dpp<XOR8>(m)));
    const float f = __builtin_amdgcn_exp2f((m - Mw) * LOG2E);
    l *= f;
#pragma unroll
    for (int k = 0; k < 24; ++k) acc[k] *= f;
    l += dpp<XOR8>(l); l = rs16(l, l); l = rs32(l, l);
    float r[3];
    reduce_scatter24(acc, r, lane);
#pragma unroll
    for (int i = 0; i < 3; ++i) OW[sw * 192 + 64 * i + 8 * c + g] = r[i];
    if (lane == 0) *(float2*)(ML + 2 * sw) = make_float2(Mw, l);
    BARRIER();
    BARRIER();
    const float* op = OP + (yrg < 3 ? yrg : 0) * 32;
    const unsigned char* wl = lds + L_WO + (t4 < 240 ? t4 : 0) * 16;
#pragma unroll
    for (int j4 = 0; j4 < 8; ++j4) {
      const f32x4 ov = *(const f32x4*)(op + 4 * j4);
#pragma unroll
      for (int u = 0; u < 4; ++u) fma8(ya, *(const u32x4*)(wl + (4 * j4 + u) * 3840), ov[u]);
    }
  }
  if (t4 < 240) {
    float* yp = YP + ((wave < 4 ? 3 : 0) + yrg) * 640 + ycc * 8;
    *(f32x4*)yp = f32x4{ya[0], ya[1], ya[2], ya[3]};
    *(f32x4*)(yp + 4) = f32x4{ya[4], ya[5], ya[6], ya[7]};
  }
  BARRIER();
  if (tid < 160) {
    const float* yp = YP + tid * 4;
    const f32x4 s = ((*(const f32x4*)(yp) + *(const f32x4*)(yp + 640)) + (*(const f32x4*)(yp + 1280) + *(const f32x4*)(yp + 1920))) +
                    (*(const f32x4*)(yp + 2560) + *(const f32x4*)(yp + 3200));
    *(f32x4*)(Y + ((size_t)(tid >> 2) * 128 + src) * 16 + (tid & 3) * 4) = s;
  }
  STAMP(4);
}

__global__ void __launch_bounds__(64)
combine(const unsigned short* __restrict__ x, const unsigned short* __restrict__ b2, const unsigned char* __restrict__ ws,
        unsigned short* __restrict__ out) {
  const int lane = threadIdx.x, b = blockIdx.x / 40, cs = blockIdx.x % 40;
  const int c4 = lane & 3, sg = lane >> 2;
  const float* Y = (const float*)(ws + WS_Y) + ((size_t)b * 40 + cs) * 128 * 16 + c4 * 4;
  const f32x4* ML = (const f32x4*)(ws + WS_ML) + b * 32;
  f32x4 ya[4], ym[4], ml[2];
#pragma unroll
  for (int j = 0; j < 2; ++j) {
    ml[j] = ML[sg + 16 * j];
#pragma unroll
    for (int hf = 0; hf < 2; ++hf) {
      ya[2 * j + hf] = *(const f32x4*)(Y + (2 * (sg + 16 * j) + hf) * 16);
      ym[2 * j + hf] = *(const f32x4*)(Y + (64 + 2 * (sg + 16 * j) + hf) * 16);
    }
  }
  const int col = cs * 16 + c4 * 4;
  const uint2 xv = *(const uint2*)(x + b * DM + col);
  const uint2 bv = *(const uint2*)(b2 + col);
  f32x4 a = {0.f, 0.f, 0.f, 0.f};
#pragma unroll
  for (int j = 0; j < 2; ++j) {
    const float M = fmaxf(ml[j][0], ml[j][2]);
    const float e0 = __builtin_amdgcn_exp2f((ml[j][0] - M) * LOG2E), e1 = __builtin_amdgcn_exp2f((ml[j][2] - M) * LOG2E);
    const float inv = 1.f / fmaf(e0, ml[j][1], e1 * ml[j][3]);
    a += ya[2 * j] * (e0 * inv) + ya[2 * j + 1] * (e1 * inv);
  }
  f32x4 mm = (ym[0] + ym[1]) + (ym[2] + ym[3]);
  float r[8] = {a[0], a[1], a[2], a[3], mm[0], mm[1], mm[2], mm[3]};
#pragma unroll
  for (int k = 0; k < 8; ++k) {
    float t = r[k];
    t += __shfl_xor(t, 4);
    t += dpp<XOR8>(t);
    t = rs16(t, t);
    r[k] = rs32(t, t);
  }
  if (lane < 4) {
    const float xf[4] = {bflo(xv.x), bfhi(xv.x), bflo(xv.y), bfhi(xv.y)};
    const float bf[4] = {bflo(bv.x), bfhi(bv.x), bflo(bv.y), bfhi(bv.y)};
    float o[4];
#pragma unroll
    for (int k = 0; k < 4; ++k) o[k] = rbf(xf[k] + rbf(r[k])) + rbf(rbf(r[4 + k]) + bf[k]);
    *(uint2*)(out + b * DM + col) = make_uint2(pkbf(o[0], o[1]), pkbf(o[2], o[3]));
  }
}

extern "C" void kernel_launch(void* const* d_in, const int* in_sizes, int n_in, void* d_out, int out_size, void* d_ws,
                              size_t ws_size, hipStream_t stream) {
  (void)in_sizes; (void)n_in; (void)out_size; (void)ws_size;
  const unsigned short* x = (const unsigned short*)d_in[0];
  attn_main<<<NWG, NTHR, 0, stream>>>(x, (const unsigned char*)d_in[1], (const unsigned char*)d_in[2],
                                      (const unsigned char*)d_in[3], (const unsigned char*)d_in[4],
                                      (const unsigned char*)d_in[6], (const unsigned short*)d_in[7],
                                      (const unsigned char*)d_in[8], (unsigned char*)d_ws);
  combine<<<NB * 40, 64, 0, stream>>>(x, (const unsigned short*)d_in[9], (const unsigned char*)d_ws, (unsigned short*)d_out);
}
```

```cpp
#define ROTMUL 149
#define TAU16 1420
#define TAUC16 1370
#define WQ_AUX 0
#define WO_AUX 0
#include <hip/hip_runtime.h>
#include <hip/hip_bf16.h>
#include <stdint.h>

typedef unsigned u32x4 __attribute__((ext_vector_type(4)));
typedef float f32x4 __attribute__((ext_vector_type(4)));
typedef __bf16 bf16x2 __attribute__((ext_vector_type(2)));
typedef __amdgpu_buffer_rsrc_t rsrc_t;
#define LAS __attribute__((address_space(3)))

constexpr int NB = 4, DM = 640, NH = 32, SK = 8192, DH = 192, FF = 1536;
constexpr int NWG = 256, NTHR = 512;
constexpr int TILE_B = 8 * DH * 2;
constexpr int TILES = SK / 2 / 8;
constexpr int TPW = TILES / 4;
#define RING 8
#define WQWIN 10
constexpr int R = RING;
constexpr float LOG2E = 1.4426950408889634f;
constexpr float SCALE = 0.07216878364870322f;
constexpr unsigned OOB = 0x40000000u;

constexpr size_t WS_Y = 0, WS_ML = (size_t)NB * 40 * 128 * 16 * 4;

constexpr int L_WO = 0;
constexpr int L_YP = L_WO + 96 * 1280;
constexpr int L_OW = L_YP + 6 * 640 * 4;
constexpr int L_OP = L_OW + 4 * 192 * 4;
constexpr int L_XN = L_OP + 192 * 4;
constexpr int L_QP = L_XN + 640 * 4;
constexpr int L_HP = L_QP + 4 * 192 * 4;
constexpr int L_ML = L_HP + 4 * 32 * 4;
constexpr int L_END = L_ML + 64;

__device__ __forceinline__ rsrc_t mkrs(const void* p, unsigned bytes) {
  return __builtin_amdgcn_make_buffer_rsrc((void*)p, 0, bytes, 0x00020000);
}
template <int AUX> __device__ __forceinline__ u32x4 bld(rsrc_t r, unsigned voff, unsigned soff) {
  return __builtin_amdgcn_raw_buffer_load_b128(r, voff, soff, AUX);
}
__device__ __forceinline__ float dot2(unsigned a, unsigned b, float c) {
  return __builtin_amdgcn_fdot2_f32_bf16(__builtin_bit_cast(bf16x2, a), __builtin_bit_cast(bf16x2, b), c, false);
}
__device__ __forceinline__ float bflo(unsigned v) { return __builtin_bit_cast(float, v << 16); }
__device__ __forceinline__ float bfhi(unsigned v) { return __builtin_bit_cast(float, v & 0xffff0000u); }
__device__ __forceinline__ float rbf(float x) { return (float)(__bf16)x; }
__device__ __forceinline__ unsigned pkbf(float lo, float hi) {
  bf16x2 v = {(__bf16)lo, (__bf16)hi};
  return __builtin_bit_cast(unsigned, v);
}
template <int CTRL> __device__ __forceinline__ float dpp(float x) {
  return __builtin_bit_cast(float, __builtin_amdgcn_mov_dpp(__builtin_bit_cast(int, x), CTRL, 0xf, 0xf, true));
}
constexpr int XOR1 = 0xB1, XOR2 = 0x4E, XOR7 = 0x141, XOR8 = 0x128;
__device__ __forceinline__ float rs32(float a, float b) {
  auto s = __builtin_amdgcn_permlane32_swap(__float_as_uint(a), __float_as_uint(b), false, false);
  return __uint_as_float(s[0]) + __uint_as_float(s[1]);
}
__device__ __forceinline__ float rs16(float a, float b) {
  auto s = __builtin_amdgcn_permlane16_swap(__float_as_uint(a), __float_as_uint(b), false, false);
  return __uint_as_float(s[0]) + __uint_as_float(s[1]);
}
__device__ __forceinline__ float rs8(float a, float b, bool up) {
  const float t1 = a + dpp<XOR8>(a), t2 = b + dpp<XOR8>(b);
  return up ? t2 : t1;
}
__device__ __forceinline__ float max_x16_32(float x) {
  auto s = __builtin_amdgcn_permlane16_swap(__float_as_uint(x), __float_as_uint(x), false, false);
  x = fmaxf(__uint_as_float(s[0]), __uint_as_float(s[1]));
  auto t = __builtin_amdgcn_permlane32_swap(__float_as_uint(x), __float_as_uint(x), false, false);
  return fmaxf(__uint_as_float(t[0]), __uint_as_float(t[1]));
}
__device__ __forceinline__ float wave_sum(float x) {
  x += dpp<XOR1>(x); x += dpp<XOR2>(x); x += dpp<XOR7>(x); x += dpp<XOR8>(x);
  x = rs16(x, x);
  return rs32(x, x);
}
__device__ __forceinline__ void reduce_scatter24(const float (&a)[24], float (&r)[3], int lane) {
#pragma unroll
  for (int i = 0; i < 3; ++i) {
    float u[4], v[2];
#pragma unroll
    for (int k = 0; k < 4; ++k) u[k] = rs32(a[i * 8 + k], a[i * 8 + k + 4]);
#pragma unroll
    for (int k = 0; k < 2; ++k) v[k] = rs16(u[k], u[k + 2]);
    r[i] = rs8(v[0], v[1], lane & 8);
  }
}
__device__ __forceinline__ void fma8(float* acc, u32x4 w, float s) {
#pragma unroll
  for (int k = 0; k < 4; ++k) {
    acc[2 * k] = fmaf(s, bflo(w[k]), acc[2 * k]);
    acc[2 * k + 1] = fmaf(s, bfhi(w[k]), acc[2 * k + 1]);
  }
}
#define LDS_FENCE() asm volatile("s_waitcnt lgkmcnt(0)" ::: "memory")
#define BARRIER() do { LDS_FENCE(); __builtin_amdgcn_s_barrier(); asm volatile("" ::: "memory"); } while (0)
#define PIN24(a) asm volatile("" : "+v"(a[0]), "+v"(a[1]), "+v"(a[2]), "+v"(a[3]), "+v"(a[4]), "+v"(a[5]), "+v"(a[6]), "+v"(a[7]), \
    "+v"(a[8]), "+v"(a[9]), "+v"(a[10]), "+v"(a[11]), "+v"(a[12]), "+v"(a[13]), "+v"(a[14]), "+v"(a[15]), \
    "+v"(a[16]), "+v"(a[17]), "+v"(a[18]), "+v"(a[19]), "+v"(a[20]), "+v"(a[21]), "+v"(a[22]), "+v"(a[23]) :: "memory")
#define T0OFF 0
#define WO_T0 2000
#define WO_DT 100
__device__ __forceinline__ int wait_until16(unsigned tb, int due16) {
  int late;
  const unsigned due = tb + (unsigned)(due16 >> 4);
  asm volatile("L_pace_%=:\n\t"
               "s_memrealtime vcc\n\t"
               "s_waitcnt lgkmcnt(0)\n\t"
               "s_sub_i32 vcc_lo, vcc_lo, %1\n\t"
               "s_cmp_lt_i32 vcc_lo, 0\n\t"
               "s_cbranch_scc0 L_done_%=\n\t"
               "s_sleep 1\n\t"
               "s_branch L_pace_%=\n"
               "L_done_%=:\n\t"
               "s_mov_b32 %0, vcc_lo"
               : "=s"(late) : "s"(due) : "scc", "vcc", "memory");
  return late;
}
#define STAMP(i) do {} while (0)

__global__ void __launch_bounds__(NTHR, 2)
attn_main(const unsigned short* __restrict__ x, const unsigned char* __restrict__ kc, const unsigned char* __restrict__ vc,
          const unsigned char* __restrict__ wq, const unsigned char* __restrict__ wo, const unsigned char* __restrict__ w1,
          const unsigned short* __restrict__ b1, const unsigned char* __restrict__ w2, unsigned char* __restrict__ ws) {
  __shared__ __attribute__((aligned(16))) unsigned char lds[L_END];
  const int tid = threadIdx.x, lane = tid & 63, wave = __builtin_amdgcn_readfirstlane(tid >> 6);
  const int bid = blockIdx.x;
  const int h = (bid & 7) * 4 + ((bid >> 3) & 3), half = (bid >> 5) & 1, b = bid >> 6;
  const int src = h * 2 + half;
  const int g = lane >> 3, c = lane & 7;
  float* const YP = (float*)(lds + L_YP);
  float* const OW = (float*)(lds + L_OW);
  float* const OP = (float*)(lds + L_OP);
  float* const XN = (float*)(lds + L_XN);
  float* const QP = (float*)(lds + L_QP);
  float* const HP = (float*)(lds + L_HP);
  float* const ML = (float*)(lds + L_ML);
  float* const Y = (float*)(ws + WS_Y) + (size_t)b * 40 * 128 * 16;
  STAMP(0);
  const unsigned tb = (unsigned)__builtin_amdgcn_s_memrealtime();
  unsigned* const CNT = (unsigned*)(lds + L_ML + 32);
  if (tid == 0) *CNT = 0;
  const int t4 = tid & 255;
  const int ycc = t4 % 80, yrg = t4 / 80;
  float ya[8];
#pragma unroll
  for (int k = 0; k < 8; ++k) ya[k] = 0.f;

#define SWAP 0
  const bool helper = SWAP ? wave >= 4 : wave < 4;
  if (helper) {
    const int hw = wave & 3;
    const rsrc_t xr = mkrs(x + b * DM, DM * 2);
    const u32x4 xa = bld<0>(xr, lane * 16, 0);
    const unsigned xb = __builtin_amdgcn_raw_buffer_load_b32(xr, 1024 + lane * 4, 0, 0);
    const rsrc_t wqr = mkrs(wq + h * (DH * 2), DM * NH * DH * 2 - h * (DH * 2));
    const unsigned wqo = (unsigned)(160 * hw + g) * (NH * DH * 2) + c * 16;
    u32x4 wb[WQWIN][3];
#pragma unroll
    for (int j = 0; j < WQWIN; ++j)
#pragma unroll
      for (int i = 0; i < 3; ++i) wb[j][i] = bld<WQ_AUX>(wqr, wqo + i * 128, j * 8 * (NH * DH * 2));
    __builtin_amdgcn_sched_barrier(0);
    float xv[10];
#pragma unroll
    for (int k = 0; k < 4; ++k) { xv[2 * k] = bflo(xa[k]); xv[2 * k + 1] = bfhi(xa[k]); }
    xv[8] = bflo(xb); xv[9] = bfhi(xb);
    float sm = 0.f;
#pragma unroll
    for (int k = 0; k < 10; ++k) sm += xv[k];
    const float mu = wave_sum(sm) * (1.f / DM);
    float sq = 0.f;
#pragma unroll
    for (int k = 0; k < 10; ++k) { const float d = xv[k] - mu; sq = fmaf(d, d, sq); }
    const float var = rbf(wave_sum(sq) * (1.f / DM));
    const float mub = rbf(mu);
    const float sd = rbf(__fsqrt_rn(rbf(var + 1e-5f)));
#pragma unroll
    for (int k = 0; k < 10; ++k) xv[k] = rbf(__fdiv_rn(rbf(xv[k] - mub), sd));
    *(f32x4*)(XN + lane * 8) = f32x4{xv[0], xv[1], xv[2], xv[3]};
    *(f32x4*)(XN + lane * 8 + 4) = f32x4{xv[4], xv[5], xv[6], xv[7]};
    *(float2*)(XN + 512 + lane * 2) = make_float2(xv[8], xv[9]);
    LDS_FENCE();
    float qa[24];
#pragma unroll
    for (int k = 0; k < 24; ++k) qa[k] = 0.f;
#pragma unroll
    for (int j = 0; j < 20; ++j) {
      const float s = XN[160 * hw + 8 * j + g];
#pragma unroll
      for (int i = 0; i < 3; ++i) fma8(qa + 8 * i, wb[j % WQWIN][i], s);
      PIN24(qa);
      __builtin_amdgcn_sched_barrier(0);
      if (j + WQWIN < 20) {
#pragma unroll
        for (int i = 0; i < 3; ++i) wb[j % WQWIN][i] = bld<WQ_AUX>(wqr, wqo + i * 128, (j + WQWIN) * 8 * (NH * DH * 2));
      }
      __builtin_amdgcn_sched_barrier(0);
    }
    {
      float r[3];
      reduce_scatter24(qa, r, lane);
#pragma unroll
      for (int i = 0; i < 3; ++i) QP[hw * 192 + 64 * i + 8 * c + g] = r[i];
    }
    STAMP(1);
    BARRIER();
    const rsrc_t w1r = mkrs(w1, DM * FF * 2);
    const unsigned w1o = (lane & 3) < 3 ? (unsigned)(160 * hw + (lane >> 2)) * (FF * 2) + src * 48 + (lane & 3) * 16 : OOB;
    u32x4 hb[10];
#pragma unroll
    for (int j = 0; j < 10; ++j) hb[j] = bld<WQ_AUX>(w1r, w1o, j * 16 * (FF * 2));
    const rsrc_t w2r = mkrs(w2 + (size_t)src * 24 * (DM * 2), 24 * DM * 2);
    const int cc2 = lane % 20, rg2 = lane / 20;
    const unsigned w2o = lane < 60 ? (unsigned)rg2 * (DM * 2) + (20 * hw + cc2) * 16 : OOB;
    u32x4 gb[8];
#pragma unroll
    for (int j = 0; j < 8; ++j) gb[j] = bld<WQ_AUX>(w2r, w2o, j * 3 * (DM * 2));
    const float b1v = lane < 24 ? bflo(b1[src * 24 + lane]) : 0.f;
    float ha[8];
#pragma unroll
    for (int k = 0; k < 8; ++k) ha[k] = 0.f;
#pragma unroll
    for (int j = 0; j < 10; ++j) fma8(ha, hb[j], XN[160 * hw + 16 * j + (lane >> 2)]);
    {
      float u[4], v[2];
#pragma unroll
      for (int k = 0; k < 4; ++k) u[k] = rs32(ha[k], ha[k + 4]);
#pragma unroll
      for (int k = 0; k < 2; ++k) v[k] = rs16(u[k], u[k + 2]);
      float r = rs8(v[0], v[1], lane & 8);
      r += __shfl_xor(r, 4);
      if ((lane & 4) == 0) HP[hw * 32 + (lane & 3) * 8 + g] = r;
    }
    LDS_FENCE();
    if (lane == 0) __hip_atomic_fetch_add(CNT, 1u, __ATOMIC_RELAXED, __HIP_MEMORY_SCOPE_WORKGROUP);
    while (__hip_atomic_load(CNT, __ATOMIC_RELAXED, __HIP_MEMORY_SCOPE_WORKGROUP) < 4u) __builtin_amdgcn_s_sleep(1);
    asm volatile("" ::: "memory");
    {
      float hv = 0.f;
      if (lane < 24) {
        hv = rbf(rbf((HP[lane] + HP[32 + lane]) + (HP[64 + lane] + HP[96 + lane])) + b1v);
        hv = hv > 0.f ? hv : 0.f;
        hv = rbf(hv * hv);
      }
      float y2[8];
#pragma unroll
      for (int k = 0; k < 8; ++k) y2[k] = 0.f;
#pragma unroll
      for (int j = 0; j < 8; ++j) fma8(y2, gb[j], __shfl(hv, (rg2 + 3 * j) & 63));
#pragma unroll
      for (int k = 0; k < 8; ++k) y2[k] = (y2[k] + __shfl(y2[k], (lane + 20) & 63)) + __shfl(y2[k], (lane + 40) & 63);
      if (lane < 20) {
        const int ccg = 20 * hw + lane;
        float* dst = Y + ((size_t)(ccg >> 1) * 128 + 64 + src) * 16 + (ccg & 1) * 8;
        *(f32x4*)dst = f32x4{y2[0], y2[1], y2[2], y2[3]};
        *(f32x4*)(dst + 4) = f32x4{y2[4], y2[5], y2[6], y2[7]};
      }
    }
    const unsigned char* woh = wo + (size_t)h * DH * (DM * 2);
#define THR_N 64
#define THR_SLEEP 0
#define THROTTLE(i) do { if (WO_DT) { wait_until16(tb, (WO_T0 + (i) * WO_DT) * 16); __builtin_amdgcn_sched_barrier(0); } } while (0)
#pragma unroll
    for (int i = 0; i < 30; ++i) {
      THROTTLE(i);
      __builtin_amdgcn_global_load_lds((const unsigned*)(woh + (hw * 30 + i) * 1024 + lane * 16),
                                       (LAS unsigned*)(lds + L_WO + (hw * 30 + i) * 1024), 16, 0, WO_AUX);
      __builtin_amdgcn_sched_barrier(0);
    }
    const rsrc_t wor = mkrs(woh + 96 * (DM * 2), 96 * DM * 2);
    const unsigned woo = t4 < 240 ? (unsigned)t4 * 16 : OOB;
    u32x4 wr[32];
#pragma unroll
    for (int j = 0; j < 32; ++j) { THROTTLE(30 + j); wr[j] = bld<WO_AUX>(wor, woo, j * 3840); __builtin_amdgcn_sched_barrier(0); }
    asm volatile("s_waitcnt vmcnt(0)" ::: "memory");
    STAMP(2);
    BARRIER();
    if (t4 < 192) {
      const float M = fmaxf(fmaxf(ML[0], ML[2]), fmaxf(ML[4], ML[6]));
      float o = 0.f, L = 0.f;
#pragma unroll
      for (int w = 0; w < 4; ++w) {
        const float e = __builtin_amdgcn_exp2f((ML[2 * w] - M) * LOG2E);
        o = fmaf(e, OW[w * 192 + t4], o);
        L = fmaf(e, ML[2 * w + 1], L);
      }
      const int rr = t4 % 96;
      OP[((t4 / 96) * 3 + rr % 3) * 32 + rr / 3] = o;
      if (t4 == 0) *(float2*)(ws + WS_ML + ((size_t)b * 64 + src) * 8) = make_float2(M, L);
    }
    BARRIER();
    const float* op = OP + (3 + (yrg < 3 ? yrg : 0)) * 32;
#pragma unroll
    for (int j4 = 0; j4 < 8; ++j4) {
      const f32x4 ov = *(const f32x4*)(op + 4 * j4);
#pragma unroll
      for (int u = 0; u < 4; ++u) fma8(ya, wr[4 * j4 + u], ov[u]);
    }
  } else {
    const int sw = wave & 3;
    const size_t base = ((size_t)(b * NH + h) * SK + (size_t)half * (SK / 2)) * (DH * 2);
    const rsrc_t krs = mkrs(kc + base, TILES * TILE_B);
    const rsrc_t vrs = mkrs(vc + base, TILES * TILE_B);
    const unsigned loff = g * (DH * 2) + c * 16;
    const int t0 = sw + (b * 64 + src) * ROTMUL;
    u32x4 kb[R][3], vb[R][3];
    int due = T0OFF * 16 + sw * (TAU16 / 4);
    int dmin = -100000;
#define PACE() do { if (TAU16) { const int d_ = TAUC16 ? max(due, dmin) : due; const int l_ = wait_until16(tb, d_); due += TAU16; dmin = d_ + l_ * 16 + TAUC16; __builtin_amdgcn_sched_barrier(0); } } while (0)
#define TOFF(s) ((unsigned)((4 * (s) + t0) & (TILES - 1)) * TILE_B)
#define LOADK(j, o) do { _Pragma("unroll") for (int i_ = 0; i_ < 3; ++i_) kb[j][i_] = bld<2>(krs, loff + i_ * 128, (o)); } while (0)
#define LOADV(j, o) do { _Pragma("unroll") for (int i_ = 0; i_ < 3; ++i_) vb[j][i_] = bld<2>(vrs, loff + i_ * 128, (o)); } while (0)
#pragma unroll
    for (int j = 0; j < R; ++j) { const unsigned o = TOFF(j); PACE(); LOADK(j, o); LOADV(j, o); __builtin_amdgcn_sched_barrier(0); }
    STAMP(1);
    BARRIER();
    u32x4 q[3];
#pragma unroll
    for (int i = 0; i < 3; ++i) {
      f32x4 s0, s1;
      const float* qp = QP + 64 * i + 8 * c;
      s0 = (*(const f32x4*)(qp) + *(const f32x4*)(qp + 192)) + (*(const f32x4*)(qp + 384) + *(const f32x4*)(qp + 576));
      s1 = (*(const f32x4*)(qp + 4) + *(const f32x4*)(qp + 196)) + (*(const f32x4*)(qp + 388) + *(const f32x4*)(qp + 580));
      q[i] = u32x4{pkbf(s0[0], s0[1]), pkbf(s0[2], s0[3]), pkbf(s1[0], s1[1]), pkbf(s1[2], s1[3])};
      asm volatile("" : "+v"(q[i]) :: "memory");
      __builtin_amdgcn_sched_barrier(0);
    }
    float m = -3.0e38f, l = 0.f, acc[24];
#pragma unroll
    for (int k = 0; k < 24; ++k) acc[k] = 0.f;
    STAMP(2);
#define TILE(j, LK, LV) do { \
      float s0_ = 0.f, s1_ = 0.f, s2_ = 0.f; \
      _Pragma("unroll") for (int k_ = 0; k_ < 4; ++k_) { \
        s0_ = dot2(kb[j][0][k_], q[0][k_], s0_); s1_ = dot2(kb[j][1][k_], q[1][k_], s1_); s2_ = dot2(kb[j][2][k_], q[2][k_], s2_); } \
      float s_ = (s0_ + s1_) + s2_; \
      asm volatile("" : "+v"(s_) :: "memory"); \
      __builtin_amdgcn_sched_barrier(0); \
      LK; \
      __builtin_amdgcn_sched_barrier(0); \
      s_ += dpp<XOR1>(s_); s_ += dpp<XOR2>(s_); s_ += dpp<XOR7>(s_); \
      s_ = rbf(rbf(s_) * SCALE); \
      const float mn_ = fmaxf(m, s_); \
      const float al_ = __builtin_amdgcn_exp2f((m - mn_) * LOG2E), p_ = __builtin_amdgcn_exp2f((s_ - mn_) * LOG2E); \
      m = mn_; l = fmaf(l, al_, p_); \
      _Pragma("unroll") for (int k_ = 0; k_ < 24; ++k_) acc[k_] *= al_; \
      _Pragma("unroll") for (int i_ = 0; i_ < 3; ++i_) fma8(acc + 8 * i_, vb[j][i_], p_); \
      PIN24(acc); \
      __builtin_amdgcn_sched_barrier(0); \
      LV; \
      __builtin_amdgcn_sched_barrier(0); \
    } while (0)
#pragma nounroll
    for (int t = R; t < TPW; t += R) {
#pragma unroll
      for (int j = 0; j < R; ++j) { const unsigned o = TOFF(t + j); TILE(j, PACE(); LOADK(j, o), LOADV(j, o)); }
    }
#pragma unroll
    for (int j = 0; j < R; ++j) TILE(j, (void)0, (void)0);
    STAMP(3);
    const float Mw = max_x16_32(fmaxf(m, dpp<XOR8>(m)));
    const float f = __builtin_amdgcn_exp2f((m - Mw) * LOG2E);
    l *= f;
#pragma unroll
    for (int k = 0; k < 24; ++k) acc[k] *= f;
    l += dpp<XOR8>(l); l = rs16(l, l); l = rs32(l, l);
    float r[3];
    reduce_scatter24(acc, r, lane);
#pragma unroll
    for (int i = 0; i < 3; ++i) OW[sw * 192 + 64 * i + 8 * c + g] = r[i];
    if (lane == 0) *(float2*)(ML + 2 * sw) = make_float2(Mw, l);
    BARRIER();
    BARRIER();
    const float* op = OP + (yrg < 3 ? yrg : 0) * 32;
    const unsigned char* wl = lds + L_WO + (t4 < 240 ? t4 : 0) * 16;
#pragma unroll
    for (int j4 = 0; j4 < 8; ++j4) {
      const f32x4 ov = *(const f32x4*)(op + 4 * j4);
#pragma unroll
      for (int u = 0; u < 4; ++u) fma8(ya, *(const u32x4*)(wl + (4 * j4 + u) * 3840), ov[u]);
    }
  }
  if (t4 < 240) {
    float* yp = YP + ((helper ? 3 : 0) + yrg) * 640 + ycc * 8;
    *(f32x4*)yp = f32x4{ya[0], ya[1], ya[2], ya[3]};
    *(f32x4*)(yp + 4) = f32x4{ya[4], ya[5], ya[6], ya[7]};
  }
  BARRIER();
  if (tid < 160) {
    const float* yp = YP + tid * 4;
    const f32x4 s = ((*(const f32x4*)(yp) + *(const f32x4*)(yp + 640)) + (*(const f32x4*)(yp + 1280) + *(const f32x4*)(yp + 1920))) +
                    (*(const f32x4*)(yp + 2560) + *(const f32x4*)(yp + 3200));
    *(f32x4*)(Y + ((size_t)(tid >> 2) * 128 + src) * 16 + (tid & 3) * 4) = s;
  }
  STAMP(4);
}

__global__ void __launch_bounds__(64)
combine(const unsigned short* __restrict__ x, const unsigned short* __restrict__ b2, const unsigned char* __restrict__ ws,
        unsigned short* __restrict__ out) {
  const int lane = threadIdx.x, b = blockIdx.x / 40, cs = blockIdx.x % 40;
  const int c4 = lane & 3, sg = lane >> 2;
  const float* Y = (const float*)(ws + WS_Y) + ((size_t)b * 40 + cs) * 128 * 16 + c4 * 4;
  const f32x4* ML = (const f32x4*)(ws + WS_ML) + b * 32;
  f32x4 ya[4], ym[4], ml[2];
#pragma unroll
  for (int j = 0; j < 2; ++j) {
    ml[j] = ML[sg + 16 * j];
#pragma unroll
    for (int hf = 0; hf < 2; ++hf) {
      ya[2 * j + hf] = *(const f32x4*)(Y + (2 * (sg + 16 * j) + hf) * 16);
      ym[2 * j + hf] = *(const f32x4*)(Y + (64 + 2 * (sg + 16 * j) + hf) * 16);
    }
  }
  const int col = cs * 16 + c4 * 4;
  const uint2 xv = *(const uint2*)(x + b * DM + col);
  const uint2 bv = *(const uint2*)(b2 + col);
  f32x4 a = {0.f, 0.f, 0.f, 0.f};
#pragma unroll
  for (int j = 0; j < 2; ++j) {
    const float M = fmaxf(ml[j][0], ml[j][2]);
    const float e0 = __builtin_amdgcn_exp2f((ml[j][0] - M) * LOG2E), e1 = __builtin_amdgcn_exp2f((ml[j][2] - M) * LOG2E);
    const float inv = 1.f / fmaf(e0, ml[j][1], e1 * ml[j][3]);
    a += ya[2 * j] * (e0 * inv) + ya[2 * j + 1] * (e1 * inv);
  }
  f32x4 mm = (ym[0] + ym[1]) + (ym[2] + ym[3]);
  float r[8] = {a[0], a[1], a[2], a[3], mm[0], mm[1], mm[2], mm[3]};
#pragma unroll
  for (int k = 0; k < 8; ++k) {
    float t = r[k];
    t += __shfl_xor(t, 4);
    t += dpp<XOR8>(t);
    t = rs16(t, t);
    r[k] = rs32(t, t);
  }
  if (lane < 4) {
    const float xf[4] = {bflo(xv.x), bfhi(xv.x), bflo(xv.y), bfhi(xv.y)};
    const float bf[4] = {bflo(bv.x), bfhi(bv.x), bflo(bv.y), bfhi(bv.y)};
    float o[4];
#pragma unroll
    for (int k = 0; k < 4; ++k) o[k] = rbf(xf[k] + rbf(r[k])) + rbf(rbf(r[4 + k]) + bf[k]);
    *(uint2*)(out + b * DM + col) = make_uint2(pkbf(o[0], o[1]), pkbf(o[2], o[3]));
  }
}

extern "C" void kernel_launch(void* const* d_in, const int* in_sizes, int n_in, void* d_out, int out_size, void* d_ws,
                              size_t ws_size, hipStream_t stream) {
  (void)in_sizes; (void)n_in; (void)out_size; (void)ws_size;
  const unsigned short* x = (const unsigned short*)d_in[0];
  attn_main<<<NWG, NTHR, 0, stream>>>(x, (const unsigned char*)d_in[1], (const unsigned char*)d_in[2],
                                      (const unsigned char*)d_in[3], (const unsigned char*)d_in[4],
                                      (const unsigned char*)d_in[6], (const unsigned short*)d_in[7],
                                      (const unsigned char*)d_in[8], (unsigned char*)d_ws);
  combine<<<NB * 40, 64, 0, stream>>>(x, (const unsigned short*)d_in[9], (const unsigned char*)d_ws, (unsigned short*)d_out);
}
```

```cpp
#define TAU16 1380
#include <hip/hip_runtime.h>
#include <hip/hip_bf16.h>
#include <stdint.h>

typedef unsigned u32x4 __attribute__((ext_vector_type(4)));
typedef unsigned u32x2 __attribute__((ext_vector_type(2)));
typedef float f32x4 __attribute__((ext_vector_type(4)));
typedef float f32x2 __attribute__((ext_vector_type(2)));
typedef __bf16 bf16x2 __attribute__((ext_vector_type(2)));
typedef __bf16 bf16x8 __attribute__((ext_vector_type(8)));
typedef short s16x4 __attribute__((ext_vector_type(4)));
typedef __amdgpu_buffer_rsrc_t rsrc_t;

constexpr int NB = 4, DM = 640, NH = 32, SK = 8192, DH = 192, FF = 1536;
constexpr int NWG = 256, NTHR = 512;
constexpr int TILE_B = 8 * DH * 2;
constexpr int PART_B = 128 * TILE_B;
#define ROTMUL 21
#define TAUC16 600
#define T0OFF 200
#define FORGIVE 100000
#define C_SLACK 1100
#define C_G 60
#define C_MISS 4800
#define C_AT 6
#define C_CAP 3200
#define WO_T0 1200
#define WO_DT 90
#define MLP_T0 1300
#define MLP_DT 100
#define LAZY 600
#define P_W0 8
#define P_W1 6
#define WQ_AUX 0
#define WO_AUX 0
#define Q_W0 24
#define Q_W1 24
#define Q_T0 480
#define WIN 0
#define T0MLP T0OFF
#define NO_MLP 0
#define NO_WO 0
#define WQ_SLP 2
#define PRE 1
#define PLAIN 1
#define DBG 0
#define Q_T1 900
#define SAUX 2
constexpr int NT = 2, SC1 = 16;
constexpr float LOG2E = 1.4426950408889634f;
constexpr float SCALE = 0.07216878364870322f;
constexpr unsigned OOB = 0x40000000u;
constexpr int SPIN_MAX = 1 << 18;

constexpr unsigned WS_EP = 0;
constexpr unsigned WS_PC = 4096;
constexpr unsigned WS_MX = WS_PC + 33 * 4096;
constexpr unsigned WS_QX = WS_MX + NB * NH * 1024;
constexpr unsigned WS_OX = WS_QX + 40 * NB * 2 * DH * 8;
constexpr unsigned WS_Y = WS_OX + NB * NH * 16 * 768;
constexpr unsigned WS_END = WS_Y + NB * 40 * NH * 128;

constexpr int L_PP = 0;
constexpr int L_QB = L_PP + 4096;
constexpr int L_OA = L_QB + 1536;
constexpr int L_YM = L_OA + 4 * 3072;
constexpr int L_CT = L_YM + 1280;
constexpr int L_END = L_CT + 64;
enum { CT_QP, CT_Q, CT_ML, CT_PO, CT_YM, CT_PY, CT_TAG, CT_OA = 8 };

__device__ __forceinline__ rsrc_t mkrs(const void* p, unsigned bytes) {
  return __builtin_amdgcn_make_buffer_rsrc((void*)p, 0, bytes, 0x00020000);
}
template <int AUX> __device__ __forceinline__ u32x4 bld(rsrc_t r, unsigned voff, unsigned soff) {
  return __builtin_amdgcn_raw_buffer_load_b128(r, voff, soff, AUX);
}
__device__ __forceinline__ void pub4(rsrc_t r, unsigned voff, u32x4 v) { __builtin_amdgcn_raw_buffer_store_b128(v, r, voff, 0, SC1); }
__device__ __forceinline__ void pub2(rsrc_t r, unsigned voff, u32x2 v) { __builtin_amdgcn_raw_buffer_store_b64(v, r, voff, 0, SC1); }
__device__ __forceinline__ float dot2(unsigned a, unsigned b, float c) {
  return __builtin_amdgcn_fdot2_f32_bf16(__builtin_bit_cast(bf16x2, a), __builtin_bit_cast(bf16x2, b), c, false);
}
__device__ __forceinline__ f32x4 mfma(u32x4 a, u32x4 b, f32x4 c) {
  return __builtin_amdgcn_mfma_f32_16x16x32_bf16(__builtin_bit_cast(bf16x8, a), __builtin_bit_cast(bf16x8, b), c, 0, 0, 0);
}
__device__ __forceinline__ f32x4 mfma16(u32x2 a, u32x2 b, f32x4 c) {
  return __builtin_amdgcn_mfma_f32_16x16x16bf16_1k(__builtin_bit_cast(s16x4, a), __builtin_bit_cast(s16x4, b), c, 0, 0, 0);
}
__device__ __forceinline__ float bflo(unsigned v) { return __builtin_bit_cast(float, v << 16); }
__device__ __forceinline__ float bfhi(unsigned v) { return __builtin_bit_cast(float, v & 0xffff0000u); }
__device__ __forceinline__ float rbf(float x) { return (float)(__bf16)x; }
__device__ __forceinline__ unsigned pkbf(float lo, float hi) {
  bf16x2 v = {(__bf16)lo, (__bf16)hi};
  return __builtin_bit_cast(unsigned, v);
}
__device__ __forceinline__ unsigned ln2(unsigned v, float mu, float rs) {
  const unsigned d = pkbf(bflo(v) - mu, bfhi(v) - mu);
  return pkbf(bflo(d) * rs, bfhi(d) * rs);
}
__device__ __forceinline__ unsigned lolo(unsigned a, unsigned b) { return __builtin_amdgcn_perm(b, a, 0x05040100u); }
__device__ __forceinline__ unsigned hihi(unsigned a, unsigned b) { return __builtin_amdgcn_perm(b, a, 0x07060302u); }
template <int CTRL> __device__ __forceinline__ float dpp(float x) {
  return __builtin_bit_cast(float, __builtin_amdgcn_mov_dpp(__builtin_bit_cast(int, x), CTRL, 0xf, 0xf, true));
}
constexpr int XOR1 = 0xB1, XOR2 = 0x4E, XOR7 = 0x141, ROR4 = 0x124, ROR8 = 0x128;
__device__ __forceinline__ float rs32(float a, float b) {
  auto s = __builtin_amdgcn_permlane32_swap(__float_as_uint(a), __float_as_uint(b), false, false);
  return __uint_as_float(s[0]) + __uint_as_float(s[1]);
}
__device__ __forceinline__ float rs16(float a, float b) {
  auto s = __builtin_amdgcn_permlane16_swap(__float_as_uint(a), __float_as_uint(b), false, false);
  return __uint_as_float(s[0]) + __uint_as_float(s[1]);
}
__device__ __forceinline__ float rs8(float a, float b, bool up) {
  const float t1 = a + dpp<ROR8>(a), t2 = b + dpp<ROR8>(b);
  return up ? t2 : t1;
}
__device__ __forceinline__ float max_x16_32(float x) {
  auto s = __builtin_amdgcn_permlane16_swap(__float_as_uint(x), __float_as_uint(x), false, false);
  x = fmaxf(__uint_as_float(s[0]), __uint_as_float(s[1]));
  auto t = __builtin_amdgcn_permlane32_swap(__float_as_uint(x), __float_as_uint(x), false, false);
  return fmaxf(__uint_as_float(t[0]), __uint_as_float(t[1]));
}
__device__ __forceinline__ float sum_q16(float x) {
  x += dpp<ROR4>(x); x += dpp<ROR8>(x);
  x = rs16(x, x);
  return rs32(x, x);
}
__device__ __forceinline__ float wave_sum(float x) {
  x += dpp<XOR1>(x); x += dpp<XOR2>(x);
  return sum_q16(x);
}
template <int SLP> __device__ __forceinline__ int wait_until(unsigned due) {
  int late;
  asm volatile("L_pace_%=:\n\t"
               "s_memrealtime vcc\n\t"
               "s_waitcnt lgkmcnt(0)\n\t"
               "s_sub_i32 vcc_lo, vcc_lo, %1\n\t"
               "s_cmp_lt_i32 vcc_lo, 0\n\t"
               "s_cbranch_scc0 L_done_%=\n\t"
               "s_sleep %2\n\t"
               "s_branch L_pace_%=\n"
               "L_done_%=:\n\t"
               "s_mov_b32 %0, vcc_lo"
               : "=s"(late) : "s"(due), "n"(SLP) : "scc", "vcc", "memory");
  return late;
}
__device__ __forceinline__ int loads_in_flight() {
  unsigned ib;
  asm volatile("s_getreg_b32 %0, hwreg(HW_REG_IB_STS)" : "=s"(ib));
  return (ib & 15) | ((ib >> 30) << 4);
}
template <int CTRL> __device__ __forceinline__ int dppi(int x) { return __builtin_amdgcn_mov_dpp(x, CTRL, 0xf, 0xf, true); }
template <int J> __device__ __forceinline__ float bcast8(float x) {
  const int v = __builtin_bit_cast(int, x);
  const int lo = __builtin_amdgcn_update_dpp(0, v, 0x150 + J, 0xf, 0x3, false);
  return __builtin_bit_cast(float, __builtin_amdgcn_update_dpp(lo, v, 0x158 + J, 0xf, 0xc, false));
}
#define SCHED() __builtin_amdgcn_sched_barrier(0)
#define LDS_FENCE() asm volatile("s_waitcnt lgkmcnt(0)" ::: "memory")
#define DRAIN() asm volatile("s_waitcnt vmcnt(0)" ::: "memory")
#define BARRIER() do { SCHED(); LDS_FENCE(); __builtin_amdgcn_s_barrier(); asm volatile("" ::: "memory"); SCHED(); } while (0)
#define STAMP(i) do {} while (0)

__device__ __forceinline__ unsigned lds_arrive(unsigned* cnt, int lane) {
  LDS_FENCE();
  unsigned old = 0;
  if (lane == 0) old = __hip_atomic_fetch_add(cnt, 1u, __ATOMIC_RELAXED, __HIP_MEMORY_SCOPE_WORKGROUP);
  old = __builtin_amdgcn_readfirstlane(old);
  asm volatile("" ::: "memory");
  return old;
}
__device__ __forceinline__ void lds_post(unsigned* cnt, int lane) {
  LDS_FENCE();
  if (lane == 0) __hip_atomic_fetch_add(cnt, 1u, __ATOMIC_RELAXED, __HIP_MEMORY_SCOPE_WORKGROUP);
  asm volatile("" ::: "memory");
}
template <int SLP> __device__ __forceinline__ void lds_wait(unsigned* cnt, unsigned n) {
  while (__builtin_amdgcn_readfirstlane(__hip_atomic_load(cnt, __ATOMIC_RELAXED, __HIP_MEMORY_SCOPE_WORKGROUP)) < n)
    __builtin_amdgcn_s_sleep(SLP);
  asm volatile("" ::: "memory");
}
__device__ __forceinline__ void lazy(int ticks) { wait_until<8>((unsigned)__builtin_amdgcn_s_memrealtime() + ticks); }
__device__ __forceinline__ unsigned bad2(u32x4 v, unsigned tag) { return (v[1] ^ tag) | (v[3] ^ tag); }
#define LOAD80(w, u, r, vo1, vo2, so) do { w = __builtin_amdgcn_raw_buffer_load_b64(r, vo1, so, WO_AUX); \
    u = __builtin_amdgcn_raw_buffer_load_b16(r, vo2, so, WO_AUX); } while (0)

__device__ __forceinline__ void fetch_q(rsrc_t wsr, unsigned char* lds, int h, int n, unsigned tag, int lane) {
  const unsigned qo = WS_QX + (h * 4 + n) * (2 * DH * 8) + lane * 16, qo2 = qo + 1024 + (lane >> 5) * 1024;
  u32x4 qa, qb, qc;
#pragma nounroll
  for (int it = 0; it < SPIN_MAX; ++it) {
    if (PLAIN && it == 0) { qa = bld<0>(wsr, qo, 0); qb = bld<0>(wsr, qo, DH * 8); qc = bld<0>(wsr, qo2, 0); }
    else { qa = bld<SC1>(wsr, qo, 0); qb = bld<SC1>(wsr, qo, DH * 8); qc = bld<SC1>(wsr, qo2, 0); }
    const unsigned bad = bad2(qa, tag) | bad2(qb, tag) | bad2(qc, tag);
    if (__builtin_amdgcn_ballot_w64(bad != 0) == 0) break;
    __builtin_amdgcn_s_sleep(Q_W1);
    asm volatile("" ::: "memory");
  }
  unsigned* QB = (unsigned*)(lds + L_QB + n * 384);
  QB[lane] = pkbf(__uint_as_float(qa[0]) + __uint_as_float(qb[0]), __uint_as_float(qa[2]) + __uint_as_float(qb[2]));
  const float c0 = __uint_as_float(qc[0]), c1 = __uint_as_float(qc[2]);
  const unsigned t = pkbf(rs32(c0, c0), rs32(c1, c1));
  if (lane < 32) QB[64 + lane] = t;
}
__device__ __forceinline__ float sum_row16(float x) {
  x += dpp<XOR1>(x); x += dpp<XOR2>(x); x += dpp<XOR7>(x);
  return x + dpp<ROR8>(x);
}

__global__ void __launch_bounds__(NTHR, 2)
fused_block(const unsigned short* __restrict__ x, const unsigned char* __restrict__ kc, const unsigned char* __restrict__ vc,
            const unsigned char* __restrict__ wq, const unsigned char* __restrict__ wo, const unsigned char* __restrict__ w1,
            const unsigned short* __restrict__ b1, const unsigned char* __restrict__ w2, const unsigned short* __restrict__ b2,
            unsigned char* ws, unsigned short* out) {
  __shared__ __attribute__((aligned(16))) unsigned char lds[L_END];
  const unsigned long long tb64 = __builtin_amdgcn_s_memrealtime();
  const unsigned tb = (unsigned)tb64;
  const int tid = threadIdx.x, lane = tid & 63, wave = __builtin_amdgcn_readfirstlane(tid >> 6);
  const int bid = blockIdx.x;
  const int h = (bid & 7) * 4 + ((bid >> 3) & 3), j8 = bid >> 5;
  unsigned* const CT = (unsigned*)(lds + L_CT);
  const rsrc_t wsr = mkrs(ws, WS_END);
  if (tid < 16) CT[tid] = 0;
  BARRIER();

  if (wave < 4) {
    const int hw = wave;
    const int kq = lane >> 4, nn = lane & 15;
    const bool mlp = j8 >= 6;
    const int half = mlp ? j8 & 1 : j8 / 3, grp = mlp ? 32 + (h >> 2) : h, sub = mlp ? h & 3 : j8 % 3, qr = half * 320 + hw * 80 + kq * 20;
    const unsigned rstr = mlp ? FF * 2 : NH * DH * 2;
    const rsrc_t xr = mkrs(x, NB * DM * 2);
    u32x4 xs[5];
    u32x2 xa[5], wq2[20];
#pragma unroll
    for (int i = 0; i < 5; ++i) xs[i] = bld<0>(xr, kq * (DM * 2) + nn * 16, i * 256);
    const rsrc_t wqr = mkrs((mlp ? w1 : wq) + (grp & 31) * 384 + sub * 128, sub < 3 ? (DM - 1) * rstr + 128 : 0);
#pragma unroll
    for (int i = 0; i < 5; ++i) xa[i] = __builtin_amdgcn_raw_buffer_load_b64(xr, (nn >> 2) * (DM * 2) + qr * 2, i * 8, 0);
    const u32x2 ep = __builtin_amdgcn_raw_buffer_load_b64(wsr, WS_EP, 0, SC1);
    const unsigned wqo = qr * rstr + nn * 8;
#pragma unroll
    for (int j = 0; j < 20; ++j) { wq2[j] = __builtin_amdgcn_raw_buffer_load_b64(wqr, wqo, j * rstr, WQ_AUX); if (WQ_SLP) { SCHED(); __builtin_amdgcn_s_sleep(WQ_SLP); SCHED(); } }
    SCHED();
    float sm = 0.f;
#pragma unroll
    for (int i = 0; i < 5; ++i)
#pragma unroll
      for (int k = 0; k < 4; ++k) sm = dot2(xs[i][k], 0x3f803f80u, sm);
    const float mean = sum_row16(sm) * (1.f / DM), mub = rbf(mean);
    f32x2 sq = {0.f, 0.f};
#pragma unroll
    for (int i = 0; i < 5; ++i)
#pragma unroll
      for (int k = 0; k < 4; ++k) {
        const f32x2 d = f32x2{bflo(xs[i][k]), bfhi(xs[i][k])} - f32x2{mean, mean};
        sq = __builtin_elementwise_fma(d, d, sq);
      }
    const float sd = rbf(__fsqrt_rn(rbf(rbf(sum_row16(sq[0] + sq[1]) * (1.f / DM)) + 1e-5f)));
    const float rsd = 1.f / sd;
    u32x2 A[5];
    {
      const float ma = __shfl(mub, (lane & 12) * 4), ra = __shfl(rsd, (lane & 12) * 4);
#pragma unroll
      for (int i = 0; i < 5; ++i) A[i] = u32x2{ln2(xa[i][0], ma, ra), ln2(xa[i][1], ma, ra)};
    }
    SCHED();
    {
      f32x4 D[4];
#pragma unroll
      for (int c = 0; c < 4; ++c) D[c] = f32x4{0.f, 0.f, 0.f, 0.f};
#pragma unroll
      for (int j = 0; j < 5; ++j) {
        const u32x2* w = wq2 + 4 * j;
        D[0] = mfma16(A[j], u32x2{lolo(w[0][0], w[1][0]), lolo(w[2][0], w[3][0])}, D[0]);
        D[1] = mfma16(A[j], u32x2{hihi(w[0][0], w[1][0]), hihi(w[2][0], w[3][0])}, D[1]);
        D[2] = mfma16(A[j], u32x2{lolo(w[0][1], w[1][1]), lolo(w[2][1], w[3][1])}, D[2]);
        D[3] = mfma16(A[j], u32x2{hihi(w[0][1], w[1][1]), hihi(w[2][1], w[3][1])}, D[3]);
      }
      *(f32x4*)(lds + L_PP + hw * 1024 + lane * 16) = f32x4{D[0][0], D[1][0], D[2][0], D[3][0]};
    }
    const unsigned long long T = (((unsigned long long)__builtin_amdgcn_readfirstlane(ep[1]) << 32) |
                                  (unsigned)__builtin_amdgcn_readfirstlane(ep[0])) + 1ull;
    const unsigned tlo = (unsigned)T, thi = (unsigned)(T >> 32), g0 = tlo << 16, g1 = (unsigned)(T >> 16);
    if (lds_arrive(CT + CT_QP, lane) == 3) {
      STAMP(7);
      const f32x4* pp = (const f32x4*)(lds + L_PP) + lane;
      const u32x4 s = __builtin_bit_cast(u32x4, (pp[0] + pp[64]) + (pp[128] + pp[192]));
      const unsigned qo = sub < 3 ? WS_QX + (((grp * 4 + kq) * 2 + half) * DH + sub * 64 + nn * 4) * 8 : OOB;
      pub4(wsr, qo, u32x4{s[0], tlo, s[1], tlo});
      pub4(wsr, qo + 16, u32x4{s[2], tlo, s[3], tlo});
      if (lane == 0) *(u32x2*)(CT + CT_TAG) = u32x2{g0, g1};
      __builtin_amdgcn_s_sleep(Q_W0);
      wait_until<2>(tb + Q_T0);
      fetch_q(wsr, lds, h, 0, tlo, lane);
      STAMP(1);
    }
    BARRIER();
    if (hw > 0) {
      wait_until<8>(tb + Q_T1);
      fetch_q(wsr, lds, h, hw, tlo, lane);
      lds_post(CT + CT_Q, lane);
    }
    if (hw == 0) {
#pragma nounroll
      for (int b = 0; b < NB; ++b) {
        lds_wait<16>(CT + CT_ML, 4 * b + 4);
        lazy(LAZY);
        u32x4 z;
#pragma nounroll
        for (int it = 0; it < SPIN_MAX; ++it) {
          if (PLAIN && it == 0) z = bld<0>(wsr, WS_MX + (b * NH + h) * 1024 + lane * 16, 0);
          else z = bld<SC1>(wsr, WS_MX + (b * NH + h) * 1024 + lane * 16, 0);
          const unsigned bad = (((z[0] ^ g0) | (z[2] ^ g0)) >> 16) | bad2(z, g1);
          if (__builtin_amdgcn_ballot_w64(bad != 0) == 0) break;
          __builtin_amdgcn_s_sleep(48);
          asm volatile("" ::: "memory");
        }
        const float v = __uint_as_float(lolo(z[0], z[2])), o = dpp<XOR1>(v);
        const float mj = lane & 1 ? o : v, lj = lane & 1 ? 0.f : o;
        float Mh = fmaxf(mj, dpp<XOR2>(mj));
        Mh = fmaxf(Mh, dpp<XOR7>(Mh));
        Mh = max_x16_32(fmaxf(Mh, dpp<ROR8>(Mh)));
        const float ej = __builtin_amdgcn_exp2f((mj - Mh) * LOG2E);
        const int wj = __float_as_int(ej / wave_sum(ej * lj));
        const float w0 = __int_as_float(__builtin_amdgcn_readlane(wj, j8 * 8)), w1_ = __int_as_float(__builtin_amdgcn_readlane(wj, j8 * 8 + 2));
        const float w2_ = __int_as_float(__builtin_amdgcn_readlane(wj, j8 * 8 + 4)), w3 = __int_as_float(__builtin_amdgcn_readlane(wj, j8 * 8 + 6));
        const f32x4* oa = (const f32x4*)(lds + L_OA + b * 3072) + (lane < 48 ? lane : 0);
        const unsigned oo = lane < 48 && !((DBG & 1) && b < 3) ? WS_OX + ((b * NH + h) * 8 + j8) * 1536 + lane * 16 : OOB;
        lds_wait<1>(CT + CT_OA + b, 4);
        if (b == 3) STAMP(3);
        const f32x4 r = (oa[0] * w0 + oa[48] * w1_) + (oa[96] * w2_ + oa[144] * w3);
        const unsigned h0 = pkbf(r[0], r[1]), h1 = pkbf(r[2], r[3]);
        pub4(wsr, oo, u32x4{h0, tlo, h1, tlo});
        pub4(wsr, oo + 768, u32x4{pkbf(r[0] - bflo(h0), r[1] - bfhi(h0)), tlo, pkbf(r[2] - bflo(h1), r[3] - bfhi(h1)), tlo});
        lds_post(CT + CT_PO, lane);
      }
    } else if (hw == 1) {
      const rsrc_t wor = mkrs(wo + (size_t)h * DH * (DM * 2) + j8 * 160, NO_WO ? 0 : (DH - 1) * (DM * 2) + 160);
      const unsigned wo1 = kq * (4 * DM * 2) + nn * 8, wo2 = kq * (4 * DM * 2) + 128 + nn * 2;
      u32x4 F[6][5];
#pragma unroll
      for (int kb = 0; kb < 6; ++kb) {
        u32x2 w[8];
        unsigned u[8];
#pragma unroll
        for (int j = 0; j < 8; ++j) {
          wait_until<8>(tb + WO_T0 + (8 * kb + j) * WO_DT);
          LOAD80(w[j], u[j], wor, wo1, wo2, (32 * kb + 16 * (j >> 2) + (j & 3)) * (DM * 2));
          SCHED();
        }
#pragma unroll
        for (int k = 0; k < 2; ++k) {
          F[kb][2 * k] = u32x4{lolo(w[0][k], w[1][k]), lolo(w[2][k], w[3][k]), lolo(w[4][k], w[5][k]), lolo(w[6][k], w[7][k])};
          F[kb][2 * k + 1] = u32x4{hihi(w[0][k], w[1][k]), hihi(w[2][k], w[3][k]), hihi(w[4][k], w[5][k]), hihi(w[6][k], w[7][k])};
        }
        F[kb][4] = u32x4{lolo(u[0], u[1]), lolo(u[2], u[3]), lolo(u[4], u[5]), lolo(u[6], u[7])};
        asm volatile("" : "+v"(F[kb][0]), "+v"(F[kb][1]), "+v"(F[kb][2]), "+v"(F[kb][3]), "+v"(F[kb][4]) :: "memory");
        SCHED();
      }
      lds_wait<8>(CT + CT_YM, 1);
#pragma nounroll
      for (int b = 0; b < NB; ++b) {
        const float* ym = (const float*)(lds + L_YM) + b * 80;
        const float m0 = ym[4 * nn + kq], m1 = ym[64 + nn];
        const unsigned oo = WS_OX + ((b * NH + h) * 16 + nn) * 768 + kq * 16;
        const unsigned yo = ((DBG & 4) && b < 3) ? OOB : WS_Y + ((b * 40 + j8 * 5) * NH + h) * 128;
        lds_wait<1>(CT + CT_PO, b + 1);
        if ((DBG & 3) && b < 3) { lds_post(CT + CT_PY, lane); continue; }
        if (b < 3) lazy(LAZY); else __builtin_amdgcn_s_sleep(P_W0);
        u32x4 z[12];
#pragma nounroll
        for (int it = 0; it < SPIN_MAX; ++it) {
          unsigned bad = 0;
          if (PLAIN && it == 0 && b < 3) {
#pragma unroll
            for (int i = 0; i < 12; ++i) z[i] = bld<0>(wsr, oo + i * 64, 0);
          } else {
#pragma unroll
            for (int i = 0; i < 12; ++i) z[i] = bld<SC1>(wsr, oo + i * 64, 0);
          }
#pragma unroll
          for (int i = 0; i < 12; ++i) bad |= bad2(z[i], tlo);
          if (__builtin_amdgcn_ballot_w64(bad != 0) == 0) break;
          __builtin_amdgcn_s_sleep(P_W1);
          asm volatile("" ::: "memory");
        }
        if (b == 3) STAMP(5);
        f32x4 D[5];
#pragma unroll
        for (int c = 0; c < 5; ++c) D[c] = f32x4{0.f, 0.f, 0.f, 0.f};
#pragma unroll
        for (int kb = 0; kb < 6; ++kb) {
          const u32x4 a = {z[2 * kb][0], z[2 * kb][2], z[2 * kb + 1][0], z[2 * kb + 1][2]};
#pragma unroll
          for (int c = 0; c < 5; ++c) D[c] = mfma(a, F[kb][c], D[c]);
        }
        float t[5];
#pragma unroll
        for (int c = 0; c < 5; ++c) t[c] = (D[c][0] + D[c][1]) + (D[c][2] + D[c][3]);
        const float y0 = rs16(rs32(t[0], t[2]), rs32(t[1], t[3])) + m0;
        t[4] = rs32(t[4], t[4]);
        const float y1 = rs16(t[4], t[4]) + m1;
        pub2(wsr, yo + (nn >> 2) * (NH * 128) + ((nn & 3) * 4 + kq) * 8, u32x2{__float_as_uint(y0), tlo});
        pub2(wsr, lane < 16 ? yo + 4 * (NH * 128) + nn * 8 : OOB, u32x2{__float_as_uint(y1), tlo});
        lds_post(CT + CT_PY, lane);
      }
    } else if (hw == 2) {
      const rsrc_t w2r = mkrs(w2 + (size_t)h * 48 * (DM * 2) + j8 * 160, NO_MLP ? 0 : 47 * (DM * 2) + 160);
      u32x2 w[12], bb[3];
      unsigned u[12];
#pragma unroll
      for (int j = 0; j < 12; ++j) {
        wait_until<8>(tb + MLP_T0 + j * MLP_DT);
        LOAD80(w[j], u[j], w2r, kq * (12 * DM * 2) + nn * 8, kq * (12 * DM * 2) + 128 + nn * 2, j * (DM * 2));
        SCHED();
      }
#pragma unroll
      for (int j = 0; j < 3; ++j) bb[j] = *(const u32x2*)(b1 + h * 48 + kq * 12 + j * 4);
      const unsigned ho = WS_QX + (((32 + (h >> 2)) * 4 + (nn >> 2)) * 2 * DH + (h & 3) * 48 + kq * 12) * 8;
      u32x4 z[12];
#pragma nounroll
      for (int it = 0; it < SPIN_MAX; ++it) {
        unsigned bad = 0;
        if (PLAIN && it == 0) {
#pragma unroll
          for (int i = 0; i < 12; ++i) z[i] = bld<0>(wsr, ho + (i % 6) * 16 + (i / 6) * (DH * 8), 0);
        } else {
#pragma unroll
          for (int i = 0; i < 12; ++i) z[i] = bld<SC1>(wsr, ho + (i % 6) * 16 + (i / 6) * (DH * 8), 0);
        }
#pragma unroll
        for (int i = 0; i < 12; ++i) bad |= bad2(z[i], tlo);
        if (__builtin_amdgcn_ballot_w64(bad != 0) == 0) break;
        __builtin_amdgcn_s_sleep(48);
        asm volatile("" ::: "memory");
      }
      f32x4 D[5];
#pragma unroll
      for (int c = 0; c < 5; ++c) D[c] = f32x4{0.f, 0.f, 0.f, 0.f};
#pragma unroll
      for (int j = 0; j < 3; ++j) {
        u32x2 a;
#pragma unroll
        for (int k = 0; k < 2; ++k) {
          const u32x4 p = z[2 * j + k], q = z[6 + 2 * j + k];
          float h0 = rbf(rbf(__uint_as_float(p[0]) + __uint_as_float(q[0])) + bflo(bb[j][k]));
          float h1 = rbf(rbf(__uint_as_float(p[2]) + __uint_as_float(q[2])) + bfhi(bb[j][k]));
          h0 = h0 > 0.f ? h0 : 0.f; h1 = h1 > 0.f ? h1 : 0.f;
          a[k] = pkbf(h0 * h0, h1 * h1);
        }
        const u32x2* v = w + 4 * j;
        D[0] = mfma16(a, u32x2{lolo(v[0][0], v[1][0]), lolo(v[2][0], v[3][0])}, D[0]);
        D[1] = mfma16(a, u32x2{hihi(v[0][0], v[1][0]), hihi(v[2][0], v[3][0])}, D[1]);
        D[2] = mfma16(a, u32x2{lolo(v[0][1], v[1][1]), lolo(v[2][1], v[3][1])}, D[2]);
        D[3] = mfma16(a, u32x2{hihi(v[0][1], v[1][1]), hihi(v[2][1], v[3][1])}, D[3]);
        D[4] = mfma16(a, u32x2{lolo(u[4 * j], u[4 * j + 1]), lolo(u[4 * j + 2], u[4 * j + 3])}, D[4]);
      }
      float* ym = (float*)(lds + L_YM) + kq * 80;
      *(f32x4*)(ym + 4 * nn) = f32x4{D[0][0], D[1][0], D[2][0], D[3][0]};
      ym[64 + nn] = D[4][0];
      lds_post(CT + CT_YM, lane);
    } else if (bid < 160 && (!(DBG & 15) || bid >= 120)) {
      const int rb = bid / 40, col = (bid % 40) * 16 + (lane & 7) * 2;
      const unsigned xv = *(const unsigned*)(x + rb * DM + col), bv = *(const unsigned*)(b2 + col);
      const float r0 = bflo(xv) + bflo(bv), r1 = bfhi(xv) + bfhi(bv);
      lds_wait<1>(CT + CT_PY, rb + 1);
      if (rb < 3) lazy(LAZY); else __builtin_amdgcn_s_sleep(P_W0);
      u32x4 z[4];
#pragma nounroll
      for (int it = 0; it < SPIN_MAX; ++it) {
#pragma unroll
        for (int i = 0; i < 4; ++i) z[i] = bld<SC1>(wsr, WS_Y + bid * (NH * 128) + lane * 16, i * 1024);
        if (__builtin_amdgcn_ballot_w64((bad2(z[0], tlo) | bad2(z[1], tlo) | bad2(z[2], tlo) | bad2(z[3], tlo)) != 0) == 0) break;
        __builtin_amdgcn_s_sleep(P_W1);
        asm volatile("" ::: "memory");
      }
      STAMP(6);
      float s0 = (__uint_as_float(z[0][0]) + __uint_as_float(z[1][0])) + (__uint_as_float(z[2][0]) + __uint_as_float(z[3][0]));
      float s1 = (__uint_as_float(z[0][2]) + __uint_as_float(z[1][2])) + (__uint_as_float(z[2][2]) + __uint_as_float(z[3][2]));
      s0 += dpp<ROR8>(s0); s1 += dpp<ROR8>(s1);
      s0 = rs16(s0, s0); s1 = rs16(s1, s1);
      s0 = rs32(s0, s0); s1 = rs32(s1, s1);
      if (lane < 8) *(unsigned*)(out + rb * DM + col) = pkbf(r0 + s0, r1 + s1);
      if (bid == ((DBG & 15) ? 120 : 0)) pub2(wsr, lane == 0 ? WS_EP : OOB, u32x2{tlo, thi});
    }
  } else {
    const int sw = wave - 4, g = lane >> 3, c = lane & 7;
    const size_t base = ((size_t)h * SK + (size_t)j8 * (SK / 8)) * (DH * 2);
    constexpr size_t BSTR = (size_t)NH * SK * DH * 2;
    const unsigned loff = g * (DH * 2) + c * 16;
    int tc = sw + (j8 * 32 + h) * ROTMUL;
    const unsigned po = (((bid >> 3) & 15) == 0 && sw == 0 && lane < 4) ? WS_PC + 4096 + ((bid & 7) * 2 + (bid >> 7)) * 256 + lane * 16 : OOB;
    const unsigned ro = lane < 16 ? WS_PC + lane * 256 : OOB;
    int nt = sw * (TAU16 / 8), dmin = -100000, vo = T0OFF * 16, sg = vo, dv = 0, tcu = TAUC16 / 2, n = 2;
    u32x4 R[16][3];
#define ISSUE(j, r, CM) do { if (WIN) asm volatile("s_waitcnt vmcnt(%0)" :: "n"(WIN) : "memory"); vo += dv; const int d_ = max(vo + nt, dmin); const unsigned o_ = (unsigned)(tc & 127) * TILE_B; \
      const int l_ = wait_until<1>(tb + (unsigned)(d_ >> 4)), f_ = loads_in_flight(); SCHED(); \
      CM; \
      _Pragma("unroll") for (int i_ = 0; i_ < 3; ++i_) R[j][i_] = bld<SAUX>(r, loff + i_ * 128, o_); SCHED(); \
      sg = (sg * 3 + (d_ + l_ * 16 - nt + f_ * (TAU16 / 6))) >> 2; nt += TAU16 / 2; dmin = d_ + l_ * 16 + tcu; tc += 4; SCHED(); } while (0)
#define KTILE(j, J, pk) do { \
      float s0_ = 0.f, s1_ = 0.f, s2_ = 0.f; \
      _Pragma("unroll") for (int k_ = 0; k_ < 4; ++k_) { \
        s0_ = dot2(R[j][0][k_], q[0][k_], s0_); s1_ = dot2(R[j][1][k_], q[1][k_], s1_); s2_ = dot2(R[j][2][k_], q[2][k_], s2_); } \
      float s_ = (s0_ + s1_) + s2_; \
      s_ += dpp<XOR1>(s_); s_ += dpp<XOR2>(s_); s_ += dpp<XOR7>(s_); \
      pk = c == (J) ? s_ : pk; \
      asm volatile("" : "+v"(pk) :: "memory"); SCHED(); } while (0)
#define VTILE(j) do { \
      const float p_ = bcast8<j>(S0); \
      _Pragma("unroll") for (int i_ = 0; i_ < 3; ++i_) _Pragma("unroll") for (int k_ = 0; k_ < 4; ++k_) \
        acc[i_ * 4 + k_] = __builtin_elementwise_fma(f32x2{bflo(R[j][i_][k_]), bfhi(R[j][i_][k_])}, f32x2{p_, p_}, acc[i_ * 4 + k_]); \
      asm volatile("" : "+v"(acc[0]), "+v"(acc[1]), "+v"(acc[2]), "+v"(acc[3]), "+v"(acc[4]), "+v"(acc[5]), \
                        "+v"(acc[6]), "+v"(acc[7]), "+v"(acc[8]), "+v"(acc[9]), "+v"(acc[10]), "+v"(acc[11]) :: "memory"); SCHED(); } while (0)
#define TRC() (void)0
#define CONTROL(j) do { \
      if ((j) == C_AT && C_G == 0) TRC(); \
      if ((j) == C_AT && C_G) { \
        const int d_ = (int)(e - tb * 16u) - vo; \
        int y_ = (d_ > -32000 && d_ < 32000) ? d_ : (n > 2 ? C_MISS : 0); \
        y_ = (y_ + dppi<XOR1>(y_)) >> 1; \
        y_ = max(y_, dppi<XOR2>(y_)); y_ = max(y_, dppi<XOR7>(y_)); y_ = max(y_, dppi<ROR8>(y_)); \
        dv = (max(0, min(__builtin_amdgcn_readfirstlane(y_) - C_SLACK, C_CAP)) * C_G) >> 11; TRC(); SCHED(); } } while (0)
#define CTLMEM(j) do { if (C_G == 0) break; \
      if ((j) == 0) e = __builtin_amdgcn_raw_buffer_load_b32(wsr, ro, n * 4096, 0); \
      if ((j) == 2) { const unsigned v_ = tb * 16u + (unsigned)sg; __builtin_amdgcn_raw_buffer_store_b128(u32x4{v_, v_, v_, v_}, wsr, po, n * 4096, SC1); } } while (0)
    {
      const rsrc_t krs = mkrs(kc + base, PART_B);
#pragma unroll
      for (int j = 0; j < 16; ++j) { if (j < PRE) vo -= T0OFF * 16; ISSUE((j + 8) & 15, krs, (void)0); if (j < PRE) vo += T0OFF * 16; }
    }
    STAMP(1);
    BARRIER();
    u32x4 q[3];
#pragma unroll
    for (int i = 0; i < 3; ++i) q[i] = *(const u32x4*)(lds + L_QB + 128 * i + 16 * c);
    const unsigned g0 = __builtin_amdgcn_readfirstlane(CT[CT_TAG]), g1 = __builtin_amdgcn_readfirstlane(CT[CT_TAG + 1]);
    STAMP(2);
    float S0 = 0.f, S1 = 0.f, S2 = 0.f, S3 = 0.f;
    {
      const rsrc_t krs = mkrs(kc + base, PART_B);
#pragma unroll
      for (int j = 0; j < 8; ++j) { KTILE(j, j, S3); ISSUE(j, krs, (void)0); KTILE(8 + j, j, S2); }
    }
    int it = 2;
#pragma nounroll
    for (int b = 0; b < NB; ++b) {
#pragma nounroll
      for (; it < 4; ++it, ++n) {
        const rsrc_t r = mkrs((it < 3 ? kc : vc) + base + b * BSTR, PART_B);
        unsigned e;
        S0 = S1; S1 = S2; S2 = S3;
#pragma unroll
        for (int j = 0; j < 8; ++j) { KTILE(j, j, S3); CONTROL(j); ISSUE(j, r, CTLMEM(j)); }
      }
      {
        S0 = rbf(rbf(S0) * SCALE); S1 = rbf(rbf(S1) * SCALE); S2 = rbf(rbf(S2) * SCALE); S3 = rbf(rbf(S3) * SCALE);
        float m = fmaxf(fmaxf(S0, S1), fmaxf(S2, S3));
        m = fmaxf(m, dpp<XOR1>(m)); m = fmaxf(m, dpp<XOR2>(m)); m = fmaxf(m, dpp<XOR7>(m));
        m = max_x16_32(fmaxf(m, dpp<ROR8>(m)));
        S0 = __builtin_amdgcn_exp2f((S0 - m) * LOG2E); S1 = __builtin_amdgcn_exp2f((S1 - m) * LOG2E);
        S2 = __builtin_amdgcn_exp2f((S2 - m) * LOG2E); S3 = __builtin_amdgcn_exp2f((S3 - m) * LOG2E);
        const float l = wave_sum((S0 + S1) + (S2 + S3));
        const unsigned u = __float_as_uint(lane & 1 ? l : m);
        pub4(wsr, lane < 2 ? WS_MX + (b * NH + h) * 1024 + (j8 * 4 + sw) * 32 + lane * 16 : OOB,
             u32x4{g0 | (u & 0xffffu), g1, g0 | (u >> 16), g1});
        lds_post(CT + CT_ML, lane);
      }
      f32x2 acc[12];
#pragma unroll
      for (int k = 0; k < 12; ++k) acc[k] = f32x2{0.f, 0.f};
#pragma nounroll
      for (it = 0; it < 4; ++it, ++n) {
        const rsrc_t r = mkrs(it < 3 ? vc + base + b * BSTR : kc + base + (b < 3 ? b + 1 : b) * BSTR, n == 31 ? 0 : PART_B);
        if (n == 31) { nt -= 1 << 24; dmin = nt; tcu = -(1 << 24); }
        unsigned e;
        VTILE(0); CONTROL(0); ISSUE(0, r, CTLMEM(0)); VTILE(1); CONTROL(1); ISSUE(1, r, CTLMEM(1));
        VTILE(2); CONTROL(2); ISSUE(2, r, CTLMEM(2)); VTILE(3); CONTROL(3); ISSUE(3, r, CTLMEM(3));
        VTILE(4); CONTROL(4); ISSUE(4, r, CTLMEM(4)); VTILE(5); CONTROL(5); ISSUE(5, r, CTLMEM(5));
        VTILE(6); CONTROL(6); ISSUE(6, r, CTLMEM(6)); VTILE(7); CONTROL(7); ISSUE(7, r, CTLMEM(7));
        S0 = S1; S1 = S2; S2 = S3;
      }
      if (b == 3) STAMP(3);
      float* const OA = (float*)(lds + L_OA + b * 3072) + sw * 192 + 8 * c + g;
#pragma unroll
      for (int i = 0; i < 3; ++i) {
        float u[4], v[2];
#pragma unroll
        for (int k = 0; k < 4; ++k) u[k] = rs32(acc[i * 4 + (k >> 1)][k & 1], acc[i * 4 + 2 + (k >> 1)][k & 1]);
#pragma unroll
        for (int k = 0; k < 2; ++k) v[k] = rs16(u[k], u[k + 2]);
        OA[64 * i] = rs8(v[0], v[1], lane & 8);
      }
      lds_post(CT + CT_OA + b, lane);
      if (b < 3) {
        lds_wait<1>(CT + CT_Q, 3);
#pragma unroll
        for (int i = 0; i < 3; ++i) q[i] = *(const u32x4*)(lds + L_QB + (b + 1) * 384 + 128 * i + 16 * c);
      }
      it = 0;
    }
  }
  STAMP(4);
}

extern "C" void kernel_launch(void* const* d_in, const int* in_sizes, int n_in, void* d_out, int out_size, void* d_ws,
                              size_t ws_size, hipStream_t stream) {
  (void)in_sizes; (void)n_in; (void)out_size; (void)ws_size;
  fused_block<<<NWG, NTHR, 0, stream>>>((const unsigned short*)d_in[0], (const unsigned char*)d_in[1],
                                        (const unsigned char*)d_in[2], (const unsigned char*)d_in[3],
                                        (const unsigned char*)d_in[4], (const unsigned char*)d_in[6],
                                        (const unsigned short*)d_in[7], (const unsigned char*)d_in[8],
                                        (const unsigned short*)d_in[9], (unsigned char*)d_ws, (unsigned short*)d_out);
}
```

```cpp
#define TAU16 1365
#define FIXMIN 15000
#define FIXMUL 0
#define FIXWB 0
#define OUTAUX 17
#include <hip/hip_runtime.h>
#include <hip/hip_bf16.h>
#include <stdint.h>

typedef unsigned u32x4 __attribute__((ext_vector_type(4)));
typedef unsigned u32x2 __attribute__((ext_vector_type(2)));
typedef float f32x4 __attribute__((ext_vector_type(4)));
typedef float f32x2 __attribute__((ext_vector_type(2)));
typedef __bf16 bf16x2 __attribute__((ext_vector_type(2)));
typedef __bf16 bf16x8 __attribute__((ext_vector_type(8)));
typedef short s16x4 __attribute__((ext_vector_type(4)));
typedef __amdgpu_buffer_rsrc_t rsrc_t;

constexpr int NB = 4, DM = 640, NH = 32, SK = 8192, DH = 192, FF = 1536;
constexpr int NWG = 256, NTHR = 512;
constexpr int TILE_B = 8 * DH * 2;
constexpr int PART_B = 128 * TILE_B;
#define ROTMUL 21
#define TAUC16 600
#define T0OFF 200
#define FORGIVE 100000
#define C_SLACK 1100
#define C_G 60
#define C_MISS 4800
#define C_AT 6
#define C_CAP 3200
#define WO_T0 1200
#define WO_DT 90
#define MLP_T0 1300
#define MLP_DT 100
#define LAZY 600
#define P_W0 8
#define P_W1 6
#define WQ_AUX 0
#define WO_AUX 0
#define Q_W0 24
#define Q_W1 24
#define Q_T0 480
#define WIN 0
#define T0MLP T0OFF
#define NO_MLP 0
#define NO_WO 0
#define WQ_SLP 2
#define PRE 1
#define PLAIN 1
#define DBG 0
#define Q_T1 900
#define SAUX 2
constexpr int NT = 2, SC1 = 16;
constexpr float LOG2E = 1.4426950408889634f;
constexpr float SCALE = 0.07216878364870322f;
constexpr unsigned OOB = 0x40000000u;
constexpr int SPIN_MAX = 1 << 18;

constexpr unsigned WS_EP = 0;
constexpr unsigned WS_PC = 4096;
constexpr unsigned WS_MX = WS_PC + 33 * 4096;
constexpr unsigned WS_QX = WS_MX + NB * NH * 1024;
constexpr unsigned WS_OX = WS_QX + 40 * NB * 2 * DH * 8;
constexpr unsigned WS_Y = WS_OX + NB * NH * 16 * 768;
constexpr unsigned WS_END = WS_Y + NB * 40 * NH * 128;

constexpr int L_PP = 0;
constexpr int L_QB = L_PP + 4096;
constexpr int L_OA = L_QB + 1536;
constexpr int L_YM = L_OA + 4 * 3072;
constexpr int L_CT = L_YM + 1280;
constexpr int L_END = L_CT + 64;
enum { CT_QP, CT_Q, CT_ML, CT_PO, CT_YM, CT_PY, CT_TAG, CT_OA = 8 };

__device__ __forceinline__ rsrc_t mkrs(const void* p, unsigned bytes) {
  return __builtin_amdgcn_make_buffer_rsrc((void*)p, 0, bytes, 0x00020000);
}
template <int AUX> __device__ __forceinline__ u32x4 bld(rsrc_t r, unsigned voff, unsigned soff) {
  return __builtin_amdgcn_raw_buffer_load_b128(r, voff, soff, AUX);
}
__device__ __forceinline__ void pub4(rsrc_t r, unsigned voff, u32x4 v) { __builtin_amdgcn_raw_buffer_store_b128(v, r, voff, 0, SC1); }
__device__ __forceinline__ void pub2(rsrc_t r, unsigned voff, u32x2 v) { __builtin_amdgcn_raw_buffer_store_b64(v, r, voff, 0, SC1); }
__device__ __forceinline__ float dot2(unsigned a, unsigned b, float c) {
  return __builtin_amdgcn_fdot2_f32_bf16(__builtin_bit_cast(bf16x2, a), __builtin_bit_cast(bf16x2, b), c, false);
}
__device__ __forceinline__ f32x4 mfma(u32x4 a, u32x4 b, f32x4 c) {
  return __builtin_amdgcn_mfma_f32_16x16x32_bf16(__builtin_bit_cast(bf16x8, a), __builtin_bit_cast(bf16x8, b), c, 0, 0, 0);
}
__device__ __forceinline__ f32x4 mfma16(u32x2 a, u32x2 b, f32x4 c) {
  return __builtin_amdgcn_mfma_f32_16x16x16bf16_1k(__builtin_bit_cast(s16x4, a), __builtin_bit_cast(s16x4, b), c, 0, 0, 0);
}
__device__ __forceinline__ float bflo(unsigned v) { return __builtin_bit_cast(float, v << 16); }
__device__ __forceinline__ float bfhi(unsigned v) { return __builtin_bit_cast(float, v & 0xffff0000u); }
__device__ __forceinline__ float rbf(float x) { return (float)(__bf16)x; }
__device__ __forceinline__ unsigned pkbf(float lo, float hi) {
  bf16x2 v = {(__bf16)lo, (__bf16)hi};
  return __builtin_bit_cast(unsigned, v);
}
__device__ __forceinline__ unsigned ln2(unsigned v, float mu, float rs) {
  const unsigned d = pkbf(bflo(v) - mu, bfhi(v) - mu);
  return pkbf(bflo(d) * rs, bfhi(d) * rs);
}
__device__ __forceinline__ unsigned lolo(unsigned a, unsigned b) { return __builtin_amdgcn_perm(b, a, 0x05040100u); }
__device__ __forceinline__ unsigned hihi(unsigned a, unsigned b) { return __builtin_amdgcn_perm(b, a, 0x07060302u); }
template <int CTRL> __device__ __forceinline__ float dpp(float x) {
  return __builtin_bit_cast(float, __builtin_amdgcn_mov_dpp(__builtin_bit_cast(int, x), CTRL, 0xf, 0xf, true));
}
constexpr int XOR1 = 0xB1, XOR2 = 0x4E, XOR7 = 0x141, ROR4 = 0x124, ROR8 = 0x128;
__device__ __forceinline__ float rs32(float a, float b) {
  auto s = __builtin_amdgcn_permlane32_swap(__float_as_uint(a), __float_as_uint(b), false, false);
  return __uint_as_float(s[0]) + __uint_as_float(s[1]);
}
__device__ __forceinline__ float rs16(float a, float b) {
  auto s = __builtin_amdgcn_permlane16_swap(__float_as_uint(a), __float_as_uint(b), false, false);
  return __uint_as_float(s[0]) + __uint_as_float(s[1]);
}
__device__ __forceinline__ float rs8(float a, float b, bool up) {
  const float t1 = a + dpp<ROR8>(a), t2 = b + dpp<ROR8>(b);
  return up ? t2 : t1;
}
__device__ __forceinline__ float max_x16_32(float x) {
  auto s = __builtin_amdgcn_permlane16_swap(__float_as_uint(x), __float_as_uint(x), false, false);
  x = fmaxf(__uint_as_float(s[0]), __uint_as_float(s[1]));
  auto t = __builtin_amdgcn_permlane32_swap(__float_as_uint(x), __float_as_uint(x), false, false);
  return fmaxf(__uint_as_float(t[0]), __uint_as_float(t[1]));
}
__device__ __forceinline__ float sum_q16(float x) {
  x += dpp<ROR4>(x); x += dpp<ROR8>(x);
  x = rs16(x, x);
  return rs32(x, x);
}
__device__ __forceinline__ float wave_sum(float x) {
  x += dpp<XOR1>(x); x += dpp<XOR2>(x);
  return sum_q16(x);
}
template <int SLP> __device__ __forceinline__ int wait_until(unsigned due) {
  int late;
  asm volatile("L_pace_%=:\n\t"
               "s_memrealtime vcc\n\t"
               "s_waitcnt lgkmcnt(0)\n\t"
               "s_sub_i32 vcc_lo, vcc_lo, %1\n\t"
               "s_cmp_lt_i32 vcc_lo, 0\n\t"
               "s_cbranch_scc0 L_done_%=\n\t"
               "s_sleep %2\n\t"
               "s_branch L_pace_%=\n"
               "L_done_%=:\n\t"
               "s_mov_b32 %0, vcc_lo"
               : "=s"(late) : "s"(due), "n"(SLP) : "scc", "vcc", "memory");
  return late;
}
__device__ __forceinline__ int loads_in_flight() {
  unsigned ib;
  asm volatile("s_getreg_b32 %0, hwreg(HW_REG_IB_STS)" : "=s"(ib));
  return (ib & 15) | ((ib >> 30) << 4);
}
template <int CTRL> __device__ __forceinline__ int dppi(int x) { return __builtin_amdgcn_mov_dpp(x, CTRL, 0xf, 0xf, true); }
template <int J> __device__ __forceinline__ float bcast8(float x) {
  const int v = __builtin_bit_cast(int, x);
  const int lo = __builtin_amdgcn_update_dpp(0, v, 0x150 + J, 0xf, 0x3, false);
  return __builtin_bit_cast(float, __builtin_amdgcn_update_dpp(lo, v, 0x158 + J, 0xf, 0xc, false));
}
#define SCHED() __builtin_amdgcn_sched_barrier(0)
#define LDS_FENCE() asm volatile("s_waitcnt lgkmcnt(0)" ::: "memory")
#define DRAIN() asm volatile("s_waitcnt vmcnt(0)" ::: "memory")
#define BARRIER() do { SCHED(); LDS_FENCE(); __builtin_amdgcn_s_barrier(); asm volatile("" ::: "memory"); SCHED(); } while (0)
#define STAMP(i) do {} while (0)

__device__ __forceinline__ unsigned lds_arrive(unsigned* cnt, int lane) {
  LDS_FENCE();
  unsigned old = 0;
  if (lane == 0) old = __hip_atomic_fetch_add(cnt, 1u, __ATOMIC_RELAXED, __HIP_MEMORY_SCOPE_WORKGROUP);
  old = __builtin_amdgcn_readfirstlane(old);
  asm volatile("" ::: "memory");
  return old;
}
__device__ __forceinline__ void lds_post(unsigned* cnt, int lane) {
  LDS_FENCE();
  if (lane == 0) __hip_atomic_fetch_add(cnt, 1u, __ATOMIC_RELAXED, __HIP_MEMORY_SCOPE_WORKGROUP);
  asm volatile("" ::: "memory");
}
template <int SLP> __device__ __forceinline__ void lds_wait(unsigned* cnt, unsigned n) {
  while (__builtin_amdgcn_readfirstlane(__hip_atomic_load(cnt, __ATOMIC_RELAXED, __HIP_MEMORY_SCOPE_WORKGROUP)) < n)
    __builtin_amdgcn_s_sleep(SLP);
  asm volatile("" ::: "memory");
}
__device__ __forceinline__ void lazy(int ticks) { wait_until<8>((unsigned)__builtin_amdgcn_s_memrealtime() + ticks); }
__device__ __forceinline__ unsigned bad2(u32x4 v, unsigned tag) { return (v[1] ^ tag) | (v[3] ^ tag); }
#define LOAD80(w, u, r, vo1, vo2, so) do { w = __builtin_amdgcn_raw_buffer_load_b64(r, vo1, so, WO_AUX); \
    u = __builtin_amdgcn_raw_buffer_load_b16(r, vo2, so, WO_AUX); } while (0)

__device__ __forceinline__ void fetch_q(rsrc_t wsr, unsigned char* lds, int h, int n, unsigned tag, int lane) {
  const unsigned qo = WS_QX + (h * 4 + n) * (2 * DH * 8) + lane * 16, qo2 = qo + 1024 + (lane >> 5) * 1024;
  u32x4 qa, qb, qc;
#pragma nounroll
  for (int it = 0; it < SPIN_MAX; ++it) {
    if (PLAIN && it == 0) { qa = bld<0>(wsr, qo, 0); qb = bld<0>(wsr, qo, DH * 8); qc = bld<0>(wsr, qo2, 0); }
    else { qa = bld<SC1>(wsr, qo, 0); qb = bld<SC1>(wsr, qo, DH * 8); qc = bld<SC1>(wsr, qo2, 0); }
    const unsigned bad = bad2(qa, tag) | bad2(qb, tag) | bad2(qc, tag);
    if (__builtin_amdgcn_ballot_w64(bad != 0) == 0) break;
    __builtin_amdgcn_s_sleep(Q_W1);
    asm volatile("" ::: "memory");
  }
  unsigned* QB = (unsigned*)(lds + L_QB + n * 384);
  QB[lane] = pkbf(__uint_as_float(qa[0]) + __uint_as_float(qb[0]), __uint_as_float(qa[2]) + __uint_as_float(qb[2]));
  const float c0 = __uint_as_float(qc[0]), c1 = __uint_as_float(qc[2]);
  const unsigned t = pkbf(rs32(c0, c0), rs32(c1, c1));
  if (lane < 32) QB[64 + lane] = t;
}
__device__ __forceinline__ float sum_row16(float x) {
  x += dpp<XOR1>(x); x += dpp<XOR2>(x); x += dpp<XOR7>(x);
  return x + dpp<ROR8>(x);
}

__global__ void __launch_bounds__(NTHR, 2)
fused_block(const unsigned short* __restrict__ x, const unsigned char* __restrict__ kc, const unsigned char* __restrict__ vc,
            const unsigned char* __restrict__ wq, const unsigned char* __restrict__ wo, const unsigned char* __restrict__ w1,
            const unsigned short* __restrict__ b1, const unsigned char* __restrict__ w2, const unsigned short* __restrict__ b2,
            unsigned char* ws, unsigned short* out) {
  __shared__ __attribute__((aligned(16))) unsigned char lds[L_END];
  const unsigned long long tb64 = __builtin_amdgcn_s_memrealtime();
  const unsigned tb = (unsigned)tb64;
  const int tid = threadIdx.x, lane = tid & 63, wave = __builtin_amdgcn_readfirstlane(tid >> 6);
  const int bid = blockIdx.x;
  const int h = (bid & 7) * 4 + ((bid >> 3) & 3), j8 = bid >> 5;
  unsigned* const CT = (unsigned*)(lds + L_CT);
  const rsrc_t wsr = mkrs(ws, WS_END);
  if (tid == 0) __builtin_amdgcn_raw_buffer_store_b32(tb, mkrs(ws + (32 << 20), 1024), bid * 4, 0, SC1);
  if (tid < 16) CT[tid] = 0;
  BARRIER();

  if (wave < 4) {
    const int hw = wave;
    const int kq = lane >> 4, nn = lane & 15;
    const bool mlp = j8 >= 6;
    const int half = mlp ? j8 & 1 : j8 / 3, grp = mlp ? 32 + (h >> 2) : h, sub = mlp ? h & 3 : j8 % 3, qr = half * 320 + hw * 80 + kq * 20;
    const unsigned rstr = mlp ? FF * 2 : NH * DH * 2;
    const rsrc_t xr = mkrs(x, NB * DM * 2);
    u32x4 xs[5];
    u32x2 xa[5], wq2[20];
#pragma unroll
    for (int i = 0; i < 5; ++i) xs[i] = bld<0>(xr, kq * (DM * 2) + nn * 16, i * 256);
    const rsrc_t wqr = mkrs((mlp ? w1 : wq) + (grp & 31) * 384 + sub * 128, sub < 3 ? (DM - 1) * rstr + 128 : 0);
#pragma unroll
    for (int i = 0; i < 5; ++i) xa[i] = __builtin_amdgcn_raw_buffer_load_b64(xr, (nn >> 2) * (DM * 2) + qr * 2, i * 8, 0);
    const u32x2 ep = __builtin_amdgcn_raw_buffer_load_b64(wsr, WS_EP, 0, SC1);
    const unsigned wqo = qr * rstr + nn * 8;
#pragma unroll
    for (int j = 0; j < 20; ++j) { wq2[j] = __builtin_amdgcn_raw_buffer_load_b64(wqr, wqo, j * rstr, WQ_AUX); if (WQ_SLP) { SCHED(); __builtin_amdgcn_s_sleep(WQ_SLP); SCHED(); } }
    SCHED();
    float sm = 0.f;
#pragma unroll
    for (int i = 0; i < 5; ++i)
#pragma unroll
      for (int k = 0; k < 4; ++k) sm = dot2(xs[i][k], 0x3f803f80u, sm);
    const float mean = sum_row16(sm) * (1.f / DM), mub = rbf(mean);
    f32x2 sq = {0.f, 0.f};
#pragma unroll
    for (int i = 0; i < 5; ++i)
#pragma unroll
      for (int k = 0; k < 4; ++k) {
        const f32x2 d = f32x2{bflo(xs[i][k]), bfhi(xs[i][k])} - f32x2{mean, mean};
        sq = __builtin_elementwise_fma(d, d, sq);
      }
    const float sd = rbf(__fsqrt_rn(rbf(rbf(sum_row16(sq[0] + sq[1]) * (1.f / DM)) + 1e-5f)));
    const float rsd = 1.f / sd;
    u32x2 A[5];
    {
      const float ma = __shfl(mub, (lane & 12) * 4), ra = __shfl(rsd, (lane & 12) * 4);
#pragma unroll
      for (int i = 0; i < 5; ++i) A[i] = u32x2{ln2(xa[i][0], ma, ra), ln2(xa[i][1], ma, ra)};
    }
    SCHED();
    {
      f32x4 D[4];
#pragma unroll
      for (int c = 0; c < 4; ++c) D[c] = f32x4{0.f, 0.f, 0.f, 0.f};
#pragma unroll
      for (int j = 0; j < 5; ++j) {
        const u32x2* w = wq2 + 4 * j;
        D[0] = mfma16(A[j], u32x2{lolo(w[0][0], w[1][0]), lolo(w[2][0], w[3][0])}, D[0]);
        D[1] = mfma16(A[j], u32x2{hihi(w[0][0], w[1][0]), hihi(w[2][0], w[3][0])}, D[1]);
        D[2] = mfma16(A[j], u32x2{lolo(w[0][1], w[1][1]), lolo(w[2][1], w[3][1])}, D[2]);
        D[3] = mfma16(A[j], u32x2{hihi(w[0][1], w[1][1]), hihi(w[2][1], w[3][1])}, D[3]);
      }
      *(f32x4*)(lds + L_PP + hw * 1024 + lane * 16) = f32x4{D[0][0], D[1][0], D[2][0], D[3][0]};
    }
    const unsigned long long T = (((unsigned long long)__builtin_amdgcn_readfirstlane(ep[1]) << 32) |
                                  (unsigned)__builtin_amdgcn_readfirstlane(ep[0])) + 1ull;
    const unsigned tlo = (unsigned)T, thi = (unsigned)(T >> 32), g0 = tlo << 16, g1 = (unsigned)(T >> 16);
    if (lds_arrive(CT + CT_QP, lane) == 3) {
      STAMP(7);
      const f32x4* pp = (const f32x4*)(lds + L_PP) + lane;
      const u32x4 s = __builtin_bit_cast(u32x4, (pp[0] + pp[64]) + (pp[128] + pp[192]));
      const unsigned qo = sub < 3 ? WS_QX + (((grp * 4 + kq) * 2 + half) * DH + sub * 64 + nn * 4) * 8 : OOB;
      pub4(wsr, qo, u32x4{s[0], tlo, s[1], tlo});
      pub4(wsr, qo + 16, u32x4{s[2], tlo, s[3], tlo});
      if (lane == 0) *(u32x2*)(CT + CT_TAG) = u32x2{g0, g1};
      __builtin_amdgcn_s_sleep(Q_W0);
      wait_until<2>(tb + Q_T0);
      fetch_q(wsr, lds, h, 0, tlo, lane);
      STAMP(1);
    }
    BARRIER();
    if (hw > 0) {
      wait_until<8>(tb + Q_T1);
      fetch_q(wsr, lds, h, hw, tlo, lane);
      lds_post(CT + CT_Q, lane);
    }
    if (hw == 0) {
#pragma nounroll
      for (int b = 0; b < NB; ++b) {
        lds_wait<16>(CT + CT_ML, 4 * b + 4);
        lazy(LAZY);
        u32x4 z;
#pragma nounroll
        for (int it = 0; it < SPIN_MAX; ++it) {
          if (PLAIN && it == 0) z = bld<0>(wsr, WS_MX + (b * NH + h) * 1024 + lane * 16, 0);
          else z = bld<SC1>(wsr, WS_MX + (b * NH + h) * 1024 + lane * 16, 0);
          const unsigned bad = (((z[0] ^ g0) | (z[2] ^ g0)) >> 16) | bad2(z, g1);
          if (__builtin_amdgcn_ballot_w64(bad != 0) == 0) break;
          __builtin_amdgcn_s_sleep(48);
          asm volatile("" ::: "memory");
        }
        const float v = __uint_as_float(lolo(z[0], z[2])), o = dpp<XOR1>(v);
        const float mj = lane & 1 ? o : v, lj = lane & 1 ? 0.f : o;
        float Mh = fmaxf(mj, dpp<XOR2>(mj));
        Mh = fmaxf(Mh, dpp<XOR7>(Mh));
        Mh = max_x16_32(fmaxf(Mh, dpp<ROR8>(Mh)));
        const float ej = __builtin_amdgcn_exp2f((mj - Mh) * LOG2E);
        const int wj = __float_as_int(ej / wave_sum(ej * lj));
        const float w0 = __int_as_float(__builtin_amdgcn_readlane(wj, j8 * 8)), w1_ = __int_as_float(__builtin_amdgcn_readlane(wj, j8 * 8 + 2));
        const float w2_ = __int_as_float(__builtin_amdgcn_readlane(wj, j8 * 8 + 4)), w3 = __int_as_float(__builtin_amdgcn_readlane(wj, j8 * 8 + 6));
        const f32x4* oa = (const f32x4*)(lds + L_OA + b * 3072) + (lane < 48 ? lane : 0);
        const unsigned oo = lane < 48 && !((DBG & 1) && b < 3) ? WS_OX + ((b * NH + h) * 8 + j8) * 1536 + lane * 16 : OOB;
        lds_wait<1>(CT + CT_OA + b, 4);
        if (b == 3) STAMP(3);
        const f32x4 r = (oa[0] * w0 + oa[48] * w1_) + (oa[96] * w2_ + oa[144] * w3);
        const unsigned h0 = pkbf(r[0], r[1]), h1 = pkbf(r[2], r[3]);
        pub4(wsr, oo, u32x4{h0, tlo, h1, tlo});
        pub4(wsr, oo + 768, u32x4{pkbf(r[0] - bflo(h0), r[1] - bfhi(h0)), tlo, pkbf(r[2] - bflo(h1), r[3] - bfhi(h1)), tlo});
        lds_post(CT + CT_PO, lane);
      }
    } else if (hw == 1) {
      const rsrc_t wor = mkrs(wo + (size_t)h * DH * (DM * 2) + j8 * 160, NO_WO ? 0 : (DH - 1) * (DM * 2) + 160);
      const unsigned wo1 = kq * (4 * DM * 2) + nn * 8, wo2 = kq * (4 * DM * 2) + 128 + nn * 2;
      u32x4 F[6][5];
#pragma unroll
      for (int kb = 0; kb < 6; ++kb) {
        u32x2 w[8];
        unsigned u[8];
#pragma unroll
        for (int j = 0; j < 8; ++j) {
          wait_until<8>(tb + WO_T0 + (8 * kb + j) * WO_DT);
          LOAD80(w[j], u[j], wor, wo1, wo2, (32 * kb + 16 * (j >> 2) + (j & 3)) * (DM * 2));
          SCHED();
        }
#pragma unroll
        for (int k = 0; k < 2; ++k) {
          F[kb][2 * k] = u32x4{lolo(w[0][k], w[1][k]), lolo(w[2][k], w[3][k]), lolo(w[4][k], w[5][k]), lolo(w[6][k], w[7][k])};
          F[kb][2 * k + 1] = u32x4{hihi(w[0][k], w[1][k]), hihi(w[2][k], w[3][k]), hihi(w[4][k], w[5][k]), hihi(w[6][k], w[7][k])};
        }
        F[kb][4] = u32x4{lolo(u[0], u[1]), lolo(u[2], u[3]), lolo(u[4], u[5]), lolo(u[6], u[7])};
        asm volatile("" : "+v"(F[kb][0]), "+v"(F[kb][1]), "+v"(F[kb][2]), "+v"(F[kb][3]), "+v"(F[kb][4]) :: "memory");
        SCHED();
      }
      lds_wait<8>(CT + CT_YM, 1);
#pragma nounroll
      for (int b = 0; b < NB; ++b) {
        const float* ym = (const float*)(lds + L_YM) + b * 80;
        const float m0 = ym[4 * nn + kq], m1 = ym[64 + nn];
        const unsigned oo = WS_OX + ((b * NH + h) * 16 + nn) * 768 + kq * 16;
        const unsigned yo = ((DBG & 4) && b < 3) ? OOB : WS_Y + ((b * 40 + j8 * 5) * NH + h) * 128;
        lds_wait<1>(CT + CT_PO, b + 1);
        if ((DBG & 3) && b < 3) { lds_post(CT + CT_PY, lane); continue; }
        if (b < 3) lazy(LAZY); else __builtin_amdgcn_s_sleep(P_W0);
        u32x4 z[12];
#pragma nounroll
        for (int it = 0; it < SPIN_MAX; ++it) {
          unsigned bad = 0;
          if (PLAIN && it == 0 && b < 3) {
#pragma unroll
            for (int i = 0; i < 12; ++i) z[i] = bld<0>(wsr, oo + i * 64, 0);
          } else {
#pragma unroll
            for (int i = 0; i < 12; ++i) z[i] = bld<SC1>(wsr, oo + i * 64, 0);
          }
#pragma unroll
          for (int i = 0; i < 12; ++i) bad |= bad2(z[i], tlo);
          if (__builtin_amdgcn_ballot_w64(bad != 0) == 0) break;
          __builtin_amdgcn_s_sleep(P_W1);
          asm volatile("" ::: "memory");
        }
        if (b == 3) STAMP(5);
        f32x4 D[5];
#pragma unroll
        for (int c = 0; c < 5; ++c) D[c] = f32x4{0.f, 0.f, 0.f, 0.f};
#pragma unroll
        for (int kb = 0; kb < 6; ++kb) {
          const u32x4 a = {z[2 * kb][0], z[2 * kb][2], z[2 * kb + 1][0], z[2 * kb + 1][2]};
#pragma unroll
          for (int c = 0; c < 5; ++c) D[c] = mfma(a, F[kb][c], D[c]);
        }
        float t[5];
#pragma unroll
        for (int c = 0; c < 5; ++c) t[c] = (D[c][0] + D[c][1]) + (D[c][2] + D[c][3]);
        const float y0 = rs16(rs32(t[0], t[2]), rs32(t[1], t[3])) + m0;
        t[4] = rs32(t[4], t[4]);
        const float y1 = rs16(t[4], t[4]) + m1;
        pub2(wsr, yo + (nn >> 2) * (NH * 128) + ((nn & 3) * 4 + kq) * 8, u32x2{__float_as_uint(y0), tlo});
        pub2(wsr, lane < 16 ? yo + 4 * (NH * 128) + nn * 8 : OOB, u32x2{__float_as_uint(y1), tlo});
        lds_post(CT + CT_PY, lane);
      }
    } else if (hw == 2) {
      const rsrc_t w2r = mkrs(w2 + (size_t)h * 48 * (DM * 2) + j8 * 160, NO_MLP ? 0 : 47 * (DM * 2) + 160);
      u32x2 w[12], bb[3];
      unsigned u[12];
#pragma unroll
      for (int j = 0; j < 12; ++j) {
        wait_until<8>(tb + MLP_T0 + j * MLP_DT);
        LOAD80(w[j], u[j], w2r, kq * (12 * DM * 2) + nn * 8, kq * (12 * DM * 2) + 128 + nn * 2, j * (DM * 2));
        SCHED();
      }
#pragma unroll
      for (int j = 0; j < 3; ++j) bb[j] = *(const u32x2*)(b1 + h * 48 + kq * 12 + j * 4);
      const unsigned ho = WS_QX + (((32 + (h >> 2)) * 4 + (nn >> 2)) * 2 * DH + (h & 3) * 48 + kq * 12) * 8;
      u32x4 z[12];
#pragma nounroll
      for (int it = 0; it < SPIN_MAX; ++it) {
        unsigned bad = 0;
        if (PLAIN && it == 0) {
#pragma unroll
          for (int i = 0; i < 12; ++i) z[i] = bld<0>(wsr, ho + (i % 6) * 16 + (i / 6) * (DH * 8), 0);
        } else {
#pragma unroll
          for (int i = 0; i < 12; ++i) z[i] = bld<SC1>(wsr, ho + (i % 6) * 16 + (i / 6) * (DH * 8), 0);
        }
#pragma unroll
        for (int i = 0; i < 12; ++i) bad |= bad2(z[i], tlo);
        if (__builtin_amdgcn_ballot_w64(bad != 0) == 0) break;
        __builtin_amdgcn_s_sleep(48);
        asm volatile("" ::: "memory");
      }
      f32x4 D[5];
#pragma unroll
      for (int c = 0; c < 5; ++c) D[c] = f32x4{0.f, 0.f, 0.f, 0.f};
#pragma unroll
      for (int j = 0; j < 3; ++j) {
        u32x2 a;
#pragma unroll
        for (int k = 0; k < 2; ++k) {
          const u32x4 p = z[2 * j + k], q = z[6 + 2 * j + k];
          float h0 = rbf(rbf(__uint_as_float(p[0]) + __uint_as_float(q[0])) + bflo(bb[j][k]));
          float h1 = rbf(rbf(__uint_as_float(p[2]) + __uint_as_float(q[2])) + bfhi(bb[j][k]));
          h0 = h0 > 0.f ? h0 : 0.f; h1 = h1 > 0.f ? h1 : 0.f;
          a[k] = pkbf(h0 * h0, h1 * h1);
        }
        const u32x2* v = w + 4 * j;
        D[0] = mfma16(a, u32x2{lolo(v[0][0], v[1][0]), lolo(v[2][0], v[3][0])}, D[0]);
        D[1] = mfma16(a, u32x2{hihi(v[0][0], v[1][0]), hihi(v[2][0], v[3][0])}, D[1]);
        D[2] = mfma16(a, u32x2{lolo(v[0][1], v[1][1]), lolo(v[2][1], v[3][1])}, D[2]);
        D[3] = mfma16(a, u32x2{hihi(v[0][1], v[1][1]), hihi(v[2][1], v[3][1])}, D[3]);
        D[4] = mfma16(a, u32x2{lolo(u[4 * j], u[4 * j + 1]), lolo(u[4 * j + 2], u[4 * j + 3])}, D[4]);
      }
      float* ym = (float*)(lds + L_YM) + kq * 80;
      *(f32x4*)(ym + 4 * nn) = f32x4{D[0][0], D[1][0], D[2][0], D[3][0]};
      ym[64 + nn] = D[4][0];
      lds_post(CT + CT_YM, lane);
    } else if (bid < 160 && (!(DBG & 15) || bid >= 120)) {
      const int rb = bid / 40, col = (bid % 40) * 16 + (lane & 7) * 2;
      const unsigned xv = *(const unsigned*)(x + rb * DM + col), bv = *(const unsigned*)(b2 + col);
      const float r0 = bflo(xv) + bflo(bv), r1 = bfhi(xv) + bfhi(bv);
      lds_wait<1>(CT + CT_PY, rb + 1);
      if (rb < 3) lazy(LAZY); else __builtin_amdgcn_s_sleep(P_W0);
      u32x4 z[4];
#pragma nounroll
      for (int it = 0; it < SPIN_MAX; ++it) {
#pragma unroll
        for (int i = 0; i < 4; ++i) z[i] = bld<SC1>(wsr, WS_Y + bid * (NH * 128) + lane * 16, i * 1024);
        if (__builtin_amdgcn_ballot_w64((bad2(z[0], tlo) | bad2(z[1], tlo) | bad2(z[2], tlo) | bad2(z[3], tlo)) != 0) == 0) break;
        __builtin_amdgcn_s_sleep(P_W1);
        asm volatile("" ::: "memory");
      }
      STAMP(6);
      float s0 = (__uint_as_float(z[0][0]) + __uint_as_float(z[1][0])) + (__uint_as_float(z[2][0]) + __uint_as_float(z[3][0]));
      float s1 = (__uint_as_float(z[0][2]) + __uint_as_float(z[1][2])) + (__uint_as_float(z[2][2]) + __uint_as_float(z[3][2]));
      s0 += dpp<ROR8>(s0); s1 += dpp<ROR8>(s1);
      s0 = rs16(s0, s0); s1 = rs16(s1, s1);
      s0 = rs32(s0, s0); s1 = rs32(s1, s1);

      __builtin_amdgcn_raw_buffer_store_b32(pkbf(r0 + s0, r1 + s1), mkrs(out, NB * DM * 2), lane < 8 ? (rb * DM + col) * 2 : OOB, 0, OUTAUX);
      if (bid == ((DBG & 15) ? 120 : 0)) pub2(wsr, lane == 0 ? WS_EP : OOB, u32x2{tlo, thi});
    }
  } else {
    const int sw = wave - 4, g = lane >> 3, c = lane & 7;
    const size_t base = ((size_t)h * SK + (size_t)j8 * (SK / 8)) * (DH * 2);
    constexpr size_t BSTR = (size_t)NH * SK * DH * 2;
    const unsigned loff = g * (DH * 2) + c * 16;
    int tc = sw + (j8 * 32 + h) * ROTMUL;
    const unsigned po = (((bid >> 3) & 15) == 0 && sw == 0 && lane < 4) ? WS_PC + 4096 + ((bid & 7) * 2 + (bid >> 7)) * 256 + lane * 16 : OOB;
    const unsigned ro = lane < 16 ? WS_PC + lane * 256 : OOB;
    int nt = sw * (TAU16 / 8), dmin = -100000, vo = T0OFF * 16, sg = vo, dv = 0, tcu = TAUC16 / 2, n = 2;
    u32x4 R[16][3];
#define ISSUE(j, r, CM) do { if (WIN) asm volatile("s_waitcnt vmcnt(%0)" :: "n"(WIN) : "memory"); vo += dv; const int d_ = max(vo + nt, dmin); const unsigned o_ = (unsigned)(tc & 127) * TILE_B; \
      const int l_ = wait_until<1>(tb + (unsigned)(d_ >> 4)), f_ = loads_in_flight(); SCHED(); \
      CM; \
      _Pragma("unroll") for (int i_ = 0; i_ < 3; ++i_) R[j][i_] = bld<SAUX>(r, loff + i_ * 128, o_); SCHED(); \
      sg = (sg * 3 + (d_ + l_ * 16 - nt + f_ * (TAU16 / 6))) >> 2; nt += TAU16 / 2; dmin = d_ + l_ * 16 + tcu; tc += 4; SCHED(); } while (0)
#define KTILE(j, J, pk) do { \
      float s0_ = 0.f, s1_ = 0.f, s2_ = 0.f; \
      _Pragma("unroll") for (int k_ = 0; k_ < 4; ++k_) { \
        s0_ = dot2(R[j][0][k_], q[0][k_], s0_); s1_ = dot2(R[j][1][k_], q[1][k_], s1_); s2_ = dot2(R[j][2][k_], q[2][k_], s2_); } \
      float s_ = (s0_ + s1_) + s2_; \
      s_ += dpp<XOR1>(s_); s_ += dpp<XOR2>(s_); s_ += dpp<XOR7>(s_); \
      pk = c == (J) ? s_ : pk; \
      asm volatile("" : "+v"(pk) :: "memory"); SCHED(); } while (0)
#define VTILE(j) do { \
      const float p_ = bcast8<j>(S0); \
      _Pragma("unroll") for (int i_ = 0; i_ < 3; ++i_) _Pragma("unroll") for (int k_ = 0; k_ < 4; ++k_) \
        acc[i_ * 4 + k_] = __builtin_elementwise_fma(f32x2{bflo(R[j][i_][k_]), bfhi(R[j][i_][k_])}, f32x2{p_, p_}, acc[i_ * 4 + k_]); \
      asm volatile("" : "+v"(acc[0]), "+v"(acc[1]), "+v"(acc[2]), "+v"(acc[3]), "+v"(acc[4]), "+v"(acc[5]), \
                        "+v"(acc[6]), "+v"(acc[7]), "+v"(acc[8]), "+v"(acc[9]), "+v"(acc[10]), "+v"(acc[11]) :: "memory"); SCHED(); } while (0)
#define TRC() (void)0
#define CONTROL(j) do { \
      if ((j) == C_AT && C_G == 0) TRC(); \
      if ((j) == C_AT && C_G) { \
        const int d_ = (int)(e - tb * 16u) - vo; \
        int y_ = (d_ > -32000 && d_ < 32000) ? d_ : (n > 2 ? C_MISS : 0); \
        y_ = (y_ + dppi<XOR1>(y_)) >> 1; \
        y_ = max(y_, dppi<XOR2>(y_)); y_ = max(y_, dppi<XOR7>(y_)); y_ = max(y_, dppi<ROR8>(y_)); \
        dv = (max(0, min(__builtin_amdgcn_readfirstlane(y_) - C_SLACK, C_CAP)) * C_G) >> 11; TRC(); SCHED(); } } while (0)
#define CTLMEM(j) do { if (C_G == 0) break; \
      if ((j) == 0) e = __builtin_amdgcn_raw_buffer_load_b32(wsr, ro, n * 4096, 0); \
      if ((j) == 2) { const unsigned v_ = tb * 16u + (unsigned)sg; __builtin_amdgcn_raw_buffer_store_b128(u32x4{v_, v_, v_, v_}, wsr, po, n * 4096, SC1); } } while (0)
    {
      const rsrc_t krs = mkrs(kc + base, PART_B);
#pragma unroll
      for (int j = 0; j < 16; ++j) { if (j < PRE) vo -= T0OFF * 16; ISSUE((j + 8) & 15, krs, (void)0); if (j < PRE) vo += T0OFF * 16; }
    }
    STAMP(1);
    BARRIER();
    u32x4 q[3];
#pragma unroll
    for (int i = 0; i < 3; ++i) q[i] = *(const u32x4*)(lds + L_QB + 128 * i + 16 * c);
    const unsigned g0 = __builtin_amdgcn_readfirstlane(CT[CT_TAG]), g1 = __builtin_amdgcn_readfirstlane(CT[CT_TAG + 1]);
    STAMP(2);
    float S0 = 0.f, S1 = 0.f, S2 = 0.f, S3 = 0.f;
    {
      const rsrc_t krs = mkrs(kc + base, PART_B);
#pragma unroll
      for (int j = 0; j < 8; ++j) { KTILE(j, j, S3); ISSUE(j, krs, (void)0); KTILE(8 + j, j, S2); }
    }
    int it = 2;
#pragma nounroll
    for (int b = 0; b < NB; ++b) {
#pragma nounroll
      for (; it < 4; ++it, ++n) {
        const rsrc_t r = mkrs((it < 3 ? kc : vc) + base + b * BSTR, PART_B);
        unsigned e;
        S0 = S1; S1 = S2; S2 = S3;
#pragma unroll
        for (int j = 0; j < 8; ++j) { KTILE(j, j, S3); CONTROL(j); ISSUE(j, r, CTLMEM(j)); }
      }
      {
        S0 = rbf(rbf(S0) * SCALE); S1 = rbf(rbf(S1) * SCALE); S2 = rbf(rbf(S2) * SCALE); S3 = rbf(rbf(S3) * SCALE);
        float m = fmaxf(fmaxf(S0, S1), fmaxf(S2, S3));
        m = fmaxf(m, dpp<XOR1>(m)); m = fmaxf(m, dpp<XOR2>(m)); m = fmaxf(m, dpp<XOR7>(m));
        m = max_x16_32(fmaxf(m, dpp<ROR8>(m)));
        S0 = __builtin_amdgcn_exp2f((S0 - m) * LOG2E); S1 = __builtin_amdgcn_exp2f((S1 - m) * LOG2E);
        S2 = __builtin_amdgcn_exp2f((S2 - m) * LOG2E); S3 = __builtin_amdgcn_exp2f((S3 - m) * LOG2E);
        const float l = wave_sum((S0 + S1) + (S2 + S3));
        const unsigned u = __float_as_uint(lane & 1 ? l : m);
        pub4(wsr, lane < 2 ? WS_MX + (b * NH + h) * 1024 + (j8 * 4 + sw) * 32 + lane * 16 : OOB,
             u32x4{g0 | (u & 0xffffu), g1, g0 | (u >> 16), g1});
        lds_post(CT + CT_ML, lane);
      }
      f32x2 acc[12];
#pragma unroll
      for (int k = 0; k < 12; ++k) acc[k] = f32x2{0.f, 0.f};
#pragma nounroll
      for (it = 0; it < 4; ++it, ++n) {
        const rsrc_t r = mkrs(it < 3 ? vc + base + b * BSTR : kc + base + (b < 3 ? b + 1 : b) * BSTR, n == 31 ? 0 : PART_B);
        if (n == 31) { nt -= 1 << 24; dmin = nt; tcu = -(1 << 24); }
        unsigned e;
        VTILE(0); CONTROL(0); ISSUE(0, r, CTLMEM(0)); VTILE(1); CONTROL(1); ISSUE(1, r, CTLMEM(1));
        VTILE(2); CONTROL(2); ISSUE(2, r, CTLMEM(2)); VTILE(3); CONTROL(3); ISSUE(3, r, CTLMEM(3));
        VTILE(4); CONTROL(4); ISSUE(4, r, CTLMEM(4)); VTILE(5); CONTROL(5); ISSUE(5, r, CTLMEM(5));
        VTILE(6); CONTROL(6); ISSUE(6, r, CTLMEM(6)); VTILE(7); CONTROL(7); ISSUE(7, r, CTLMEM(7));
        S0 = S1; S1 = S2; S2 = S3;
      }
      if (b == 3) STAMP(3);
      float* const OA = (float*)(lds + L_OA + b * 3072) + sw * 192 + 8 * c + g;
#pragma unroll
      for (int i = 0; i < 3; ++i) {
        float u[4], v[2];
#pragma unroll
        for (int k = 0; k < 4; ++k) u[k] = rs32(acc[i * 4 + (k >> 1)][k & 1], acc[i * 4 + 2 + (k >> 1)][k & 1]);
#pragma unroll
        for (int k = 0; k < 2; ++k) v[k] = rs16(u[k], u[k + 2]);
        OA[64 * i] = rs8(v[0], v[1], lane & 8);
      }
      lds_post(CT + CT_OA + b, lane);
      if (b < 3) {
        lds_wait<1>(CT + CT_Q, 3);
#pragma unroll
        for (int i = 0; i < 3; ++i) q[i] = *(const u32x4*)(lds + L_QB + (b + 1) * 384 + 128 * i + 16 * c);
      }
      it = 0;
    }
  }
  if (wave == 0) {
    const rsrc_t tr = mkrs(ws + (32 << 20), 1024);
    const u32x4 v = bld<SC1>(tr, lane * 16, 0);
    int mn = 0, mx = 0;
    for (int i = 0; i < 4; ++i) { int d = (int)(v[i] - tb); if (d < -5000 || d > 5000) d = 0; mn = min(mn, d); mx = max(mx, d); }
    for (int o = 1; o < 64; o <<= 1) { mn = min(mn, __shfl_xor(mn, o)); mx = max(mx, __shfl_xor(mx, o)); }
    mn = __builtin_amdgcn_readfirstlane(mn); mx = __builtin_amdgcn_readfirstlane(mx);
    wait_until<1>(tb + mn + FIXMIN + (mx - mn) * FIXMUL);
  }
  STAMP(4);
}

extern "C" void kernel_launch(void* const* d_in, const int* in_sizes, int n_in, void* d_out, int out_size, void* d_ws,
                              size_t ws_size, hipStream_t stream) {
  (void)in_sizes; (void)n_in; (void)out_size; (void)ws_size;
  fused_block<<<NWG, NTHR, 0, stream>>>((const unsigned short*)d_in[0], (const unsigned char*)d_in[1],
                                        (const unsigned char*)d_in[2], (const unsigned char*)d_in[3],
                                        (const unsigned char*)d_in[4], (const unsigned char*)d_in[6],
                                        (const unsigned short*)d_in[7], (const unsigned char*)d_in[8],
                                        (const unsigned short*)d_in[9], (unsigned char*)d_ws, (unsigned short*)d_out);
}
```

```cpp
#define TAU16 1365
#include <hip/hip_runtime.h>
#include <hip/hip_bf16.h>
#include <stdint.h>

typedef unsigned u32x4 __attribute__((ext_vector_type(4)));
typedef unsigned u32x2 __attribute__((ext_vector_type(2)));
typedef float f32x4 __attribute__((ext_vector_type(4)));
typedef float f32x2 __attribute__((ext_vector_type(2)));
typedef __bf16 bf16x2 __attribute__((ext_vector_type(2)));
typedef __bf16 bf16x8 __attribute__((ext_vector_type(8)));
typedef short s16x4 __attribute__((ext_vector_type(4)));
typedef __amdgpu_buffer_rsrc_t rsrc_t;

constexpr int NB = 4, DM = 640, NH = 32, SK = 8192, DH = 192, FF = 1536;
constexpr int NWG = 256, NTHR = 512;
constexpr int TILE_B = 8 * DH * 2;
constexpr int PART_B = 128 * TILE_B;
#define ROTMUL 21
#define TAUC16 600
#define T0OFF 200
#define FORGIVE 100000
#define C_SLACK 1100
#define C_G 60
#define C_MISS 4800
#define C_AT 6
#define C_CAP 3200
#define WO_T0 1200
#define WO_DT 90
#define MLP_T0 1300
#define MLP_DT 100
#define LAZY 600
#define P_W0 8
#define P_W1 6
#define WQ_AUX 0
#define WO_AUX 0
#define Q_W0 24
#define Q_W1 24
#define Q_T0 480
#define WIN 0
#define T0MLP T0OFF
#define NO_MLP 0
#define NO_WO 0
#define WQ_SLP 2
#define PRE 1
#define PLAIN 1
#define DBG 0
#define Q_T1 900
#define SAUX 2
constexpr int NT = 2, SC1 = 16;
constexpr float LOG2E = 1.4426950408889634f;
constexpr float SCALE = 0.07216878364870322f;
constexpr unsigned OOB = 0x40000000u;
constexpr int SPIN_MAX = 1 << 18;

constexpr unsigned WS_EP = 0;
constexpr unsigned WS_PC = 4096;
constexpr unsigned WS_MX = WS_PC + 33 * 4096;
constexpr unsigned WS_QX = WS_MX + NB * NH * 1024;
constexpr unsigned WS_OX = WS_QX + 40 * NB * 2 * DH * 8;
constexpr unsigned WS_Y = WS_OX + NB * NH * 16 * 768;
constexpr unsigned WS_END = WS_Y + NB * 40 * NH * 128;

constexpr int L_PP = 0;
constexpr int L_QB = L_PP + 4096;
constexpr int L_OA = L_QB + 1536;
constexpr int L_YM = L_OA + 4 * 3072;
constexpr int L_CT = L_YM + 1280;
constexpr int L_END = L_CT + 64;
enum { CT_QP, CT_Q, CT_ML, CT_PO, CT_YM, CT_PY, CT_TAG, CT_OA = 8 };

__device__ __forceinline__ rsrc_t mkrs(const void* p, unsigned bytes) {
  return __builtin_amdgcn_make_buffer_rsrc((void*)p, 0, bytes, 0x00020000);
}
template <int AUX> __device__ __forceinline__ u32x4 bld(rsrc_t r, unsigned voff, unsigned soff) {
  return __builtin_amdgcn_raw_buffer_load_b128(r, voff, soff, AUX);
}
__device__ __forceinline__ void pub4(rsrc_t r, unsigned voff, u32x4 v) { __builtin_amdgcn_raw_buffer_store_b128(v, r, voff, 0, SC1); }
__device__ __forceinline__ void pub2(rsrc_t r, unsigned voff, u32x2 v) { __builtin_amdgcn_raw_buffer_store_b64(v, r, voff, 0, SC1); }
__device__ __forceinline__ float dot2(unsigned a, unsigned b, float c) {
  return __builtin_amdgcn_fdot2_f32_bf16(__builtin_bit_cast(bf16x2, a), __builtin_bit_cast(bf16x2, b), c, false);
}
__device__ __forceinline__ f32x4 mfma(u32x4 a, u32x4 b, f32x4 c) {
  return __builtin_amdgcn_mfma_f32_16x16x32_bf16(__builtin_bit_cast(bf16x8, a), __builtin_bit_cast(bf16x8, b), c, 0, 0, 0);
}
__device__ __forceinline__ f32x4 mfma16(u32x2 a, u32x2 b, f32x4 c) {
  return __builtin_amdgcn_mfma_f32_16x16x16bf16_1k(__builtin_bit_cast(s16x4, a), __builtin_bit_cast(s16x4, b), c, 0, 0, 0);
}
__device__ __forceinline__ float bflo(unsigned v) { return __builtin_bit_cast(float, v << 16); }
__device__ __forceinline__ float bfhi(unsigned v) { return __builtin_bit_cast(float, v & 0xffff0000u); }
__device__ __forceinline__ float rbf(float x) { return (float)(__bf16)x; }
__device__ __forceinline__ unsigned pkbf(float lo, float hi) {
  bf16x2 v = {(__bf16)lo, (__bf16)hi};
  return __builtin_bit_cast(unsigned, v);
}
__device__ __forceinline__ unsigned ln2(unsigned v, float mu, float rs) {
  const unsigned d = pkbf(bflo(v) - mu, bfhi(v) - mu);
  return pkbf(bflo(d) * rs, bfhi(d) * rs);
}
__device__ __forceinline__ unsigned lolo(unsigned a, unsigned b) { return __builtin_amdgcn_perm(b, a, 0x05040100u); }
__device__ __forceinline__ unsigned hihi(unsigned a, unsigned b) { return __builtin_amdgcn_perm(b, a, 0x07060302u); }
template <int CTRL> __device__ __forceinline__ float dpp(float x) {
  return __builtin_bit_cast(float, __builtin_amdgcn_mov_dpp(__builtin_bit_cast(int, x), CTRL, 0xf, 0xf, true));
}
constexpr int XOR1 = 0xB1, XOR2 = 0x4E, XOR7 = 0x141, ROR4 = 0x124, ROR8 = 0x128;
__device__ __forceinline__ float rs32(float a, float b) {
  auto s = __builtin_amdgcn_permlane32_swap(__float_as_uint(a), __float_as_uint(b), false, false);
  return __uint_as_float(s[0]) + __uint_as_float(s[1]);
}
__device__ __forceinline__ float rs16(float a, float b) {
  auto s = __builtin_amdgcn_permlane16_swap(__float_as_uint(a), __float_as_uint(b), false, false);
  return __uint_as_float(s[0]) + __uint_as_float(s[1]);
}
__device__ __forceinline__ float rs8(float a, float b, bool up) {
  const float t1 = a + dpp<ROR8>(a), t2 = b + dpp<ROR8>(b);
  return up ? t2 : t1;
}
__device__ __forceinline__ float max_x16_32(float x) {
  auto s = __builtin_amdgcn_permlane16_swap(__float_as_uint(x), __float_as_uint(x), false, false);
  x = fmaxf(__uint_as_float(s[0]), __uint_as_float(s[1]));
  auto t = __builtin_amdgcn_permlane32_swap(__float_as_uint(x), __float_as_uint(x), false, false);
  return fmaxf(__uint_as_float(t[0]), __uint_as_float(t[1]));
}
__device__ __forceinline__ float sum_q16(float x) {
  x += dpp<ROR4>(x); x += dpp<ROR8>(x);
  x = rs16(x, x);
  return rs32(x, x);
}
__device__ __forceinline__ float wave_sum(float x) {
  x += dpp<XOR1>(x); x += dpp<XOR2>(x);
  return sum_q16(x);
}
template <int SLP> __device__ __forceinline__ int wait_until(unsigned due) {
  int late;
  asm volatile("L_pace_%=:\n\t"
               "s_memrealtime vcc\n\t"
               "s_waitcnt lgkmcnt(0)\n\t"
               "s_sub_i32 vcc_lo, vcc_lo, %1\n\t"
               "s_cmp_lt_i32 vcc_lo, 0\n\t"
               "s_cbranch_scc0 L_done_%=\n\t"
               "s_sleep %2\n\t"
               "s_branch L_pace_%=\n"
               "L_done_%=:\n\t"
               "s_mov_b32 %0, vcc_lo"
               : "=s"(late) : "s"(due), "n"(SLP) : "scc", "vcc", "memory");
  return late;
}
__device__ __forceinline__ int loads_in_flight() {
  unsigned ib;
  asm volatile("s_getreg_b32 %0, hwreg(HW_REG_IB_STS)" : "=s"(ib));
  return (ib & 15) | ((ib >> 30) << 4);
}
template <int CTRL> __device__ __forceinline__ int dppi(int x) { return __builtin_amdgcn_mov_dpp(x, CTRL, 0xf, 0xf, true); }
template <int J> __device__ __forceinline__ float bcast8(float x) {
  const int v = __builtin_bit_cast(int, x);
  const int lo = __builtin_amdgcn_update_dpp(0, v, 0x150 + J, 0xf, 0x3, false);
  return __builtin_bit_cast(float, __builtin_amdgcn_update_dpp(lo, v, 0x158 + J, 0xf, 0xc, false));
}
#define SCHED() __builtin_amdgcn_sched_barrier(0)
#define LDS_FENCE() asm volatile("s_waitcnt lgkmcnt(0)" ::: "memory")
#define DRAIN() asm volatile("s_waitcnt vmcnt(0)" ::: "memory")
#define BARRIER() do { SCHED(); LDS_FENCE(); __builtin_amdgcn_s_barrier(); asm volatile("" ::: "memory"); SCHED(); } while (0)
#define STAMP(i) do {} while (0)

__device__ __forceinline__ unsigned lds_arrive(unsigned* cnt, int lane) {
  LDS_FENCE();
  unsigned old = 0;
  if (lane == 0) old = __hip_atomic_fetch_add(cnt, 1u, __ATOMIC_RELAXED, __HIP_MEMORY_SCOPE_WORKGROUP);
  old = __builtin_amdgcn_readfirstlane(old);
  asm volatile("" ::: "memory");
  return old;
}
__device__ __forceinline__ void lds_post(unsigned* cnt, int lane) {
  LDS_FENCE();
  if (lane == 0) __hip_atomic_fetch_add(cnt, 1u, __ATOMIC_RELAXED, __HIP_MEMORY_SCOPE_WORKGROUP);
  asm volatile("" ::: "memory");
}
template <int SLP> __device__ __forceinline__ void lds_wait(unsigned* cnt, unsigned n) {
  while (__builtin_amdgcn_readfirstlane(__hip_atomic_load(cnt, __ATOMIC_RELAXED, __HIP_MEMORY_SCOPE_WORKGROUP)) < n)
    __builtin_amdgcn_s_sleep(SLP);
  asm volatile("" ::: "memory");
}
__device__ __forceinline__ void lazy(int ticks) { wait_until<8>((unsigned)__builtin_amdgcn_s_memrealtime() + ticks); }
__device__ __forceinline__ unsigned bad2(u32x4 v, unsigned tag) { return (v[1] ^ tag) | (v[3] ^ tag); }
#define LOAD80(w, u, r, vo1, vo2, so) do { w = __builtin_amdgcn_raw_buffer_load_b64(r, vo1, so, WO_AUX); \
    u = __builtin_amdgcn_raw_buffer_load_b16(r, vo2, so, WO_AUX); } while (0)

__device__ __forceinline__ void fetch_q(rsrc_t wsr, unsigned char* lds, int h, int n, unsigned tag, int lane) {
  const unsigned qo = WS_QX + (h * 4 + n) * (2 * DH * 8) + lane * 16, qo2 = qo + 1024 + (lane >> 5) * 1024;
  u32x4 qa, qb, qc;
#pragma nounroll
  for (int it = 0; it < SPIN_MAX; ++it) {
    if (PLAIN && it == 0) { qa = bld<0>(wsr, qo, 0); qb = bld<0>(wsr, qo, DH * 8); qc = bld<0>(wsr, qo2, 0); }
    else { qa = bld<SC1>(wsr, qo, 0); qb = bld<SC1>(wsr, qo, DH * 8); qc = bld<SC1>(wsr, qo2, 0); }
    const unsigned bad = bad2(qa, tag) | bad2(qb, tag) | bad2(qc, tag);
    if (__builtin_amdgcn_ballot_w64(bad != 0) == 0) break;
    __builtin_amdgcn_s_sleep(Q_W1);
    asm volatile("" ::: "memory");
  }
  unsigned* QB = (unsigned*)(lds + L_QB + n * 384);
  QB[lane] = pkbf(__uint_as_float(qa[0]) + __uint_as_float(qb[0]), __uint_as_float(qa[2]) + __uint_as_float(qb[2]));
  const float c0 = __uint_as_float(qc[0]), c1 = __uint_as_float(qc[2]);
  const unsigned t = pkbf(rs32(c0, c0), rs32(c1, c1));
  if (lane < 32) QB[64 + lane] = t;
}
__device__ __forceinline__ float sum_row16(float x) {
  x += dpp<XOR1>(x); x += dpp<XOR2>(x); x += dpp<XOR7>(x);
  return x + dpp<ROR8>(x);
}

__global__ void __launch_bounds__(NTHR, 2)
fused_block(const unsigned short* __restrict__ x, const unsigned char* __restrict__ kc, const unsigned char* __restrict__ vc,
            const unsigned char* __restrict__ wq, const unsigned char* __restrict__ wo, const unsigned char* __restrict__ w1,
            const unsigned short* __restrict__ b1, const unsigned char* __restrict__ w2, const unsigned short* __restrict__ b2,
            unsigned char* ws, unsigned short* out) {
  __shared__ __attribute__((aligned(16))) unsigned char lds[L_END];
  const unsigned long long tb64 = __builtin_amdgcn_s_memrealtime();
  const unsigned tb = (unsigned)tb64;
  const int tid = threadIdx.x, lane = tid & 63, wave = __builtin_amdgcn_readfirstlane(tid >> 6);
  const int bid = blockIdx.x;
  const int h = (bid & 7) * 4 + ((bid >> 3) & 3), j8 = bid >> 5;
  unsigned* const CT = (unsigned*)(lds + L_CT);
  const rsrc_t wsr = mkrs(ws, WS_END);
  if (tid < 16) CT[tid] = 0;
  BARRIER();

  if (wave < 4) {
    const int hw = wave;
    const int kq = lane >> 4, nn = lane & 15;
    const bool mlp = j8 >= 6;
    const int half = mlp ? j8 & 1 : j8 / 3, grp = mlp ? 32 + (h >> 2) : h, sub = mlp ? h & 3 : j8 % 3, qr = half * 320 + hw * 80 + kq * 20;
    const unsigned rstr = mlp ? FF * 2 : NH * DH * 2;
    const rsrc_t xr = mkrs(x, NB * DM * 2);
    u32x4 xs[5];
    u32x2 xa[5], wq2[20];
#pragma unroll
    for (int i = 0; i < 5; ++i) xs[i] = bld<0>(xr, kq * (DM * 2) + nn * 16, i * 256);
    const rsrc_t wqr = mkrs((mlp ? w1 : wq) + (grp & 31) * 384 + sub * 128, sub < 3 ? (DM - 1) * rstr + 128 : 0);
#pragma unroll
    for (int i = 0; i < 5; ++i) xa[i] = __builtin_amdgcn_raw_buffer_load_b64(xr, (nn >> 2) * (DM * 2) + qr * 2, i * 8, 0);
    const u32x2 ep = __builtin_amdgcn_raw_buffer_load_b64(wsr, WS_EP, 0, SC1);
    const unsigned wqo = qr * rstr + nn * 8;
#pragma unroll
    for (int j = 0; j < 20; ++j) { wq2[j] = __builtin_amdgcn_raw_buffer_load_b64(wqr, wqo, j * rstr, WQ_AUX); if (WQ_SLP) { SCHED(); __builtin_amdgcn_s_sleep(WQ_SLP); SCHED(); } }
    SCHED();
    float sm = 0.f;
#pragma unroll
    for (int i = 0; i < 5; ++i)
#pragma unroll
      for (int k = 0; k < 4; ++k) sm = dot2(xs[i][k], 0x3f803f80u, sm);
    const float mean = sum_row16(sm) * (1.f / DM), mub = rbf(mean);
    f32x2 sq = {0.f, 0.f};
#pragma unroll
    for (int i = 0; i < 5; ++i)
#pragma unroll
      for (int k = 0; k < 4; ++k) {
        const f32x2 d = f32x2{bflo(xs[i][k]), bfhi(xs[i][k])} - f32x2{mean, mean};
        sq = __builtin_elementwise_fma(d, d, sq);
      }
    const float sd = rbf(__fsqrt_rn(rbf(rbf(sum_row16(sq[0] + sq[1]) * (1.f / DM)) + 1e-5f)));
    const float rsd = 1.f / sd;
    u32x2 A[5];
    {
      const float ma = __shfl(mub, (lane & 12) * 4), ra = __shfl(rsd, (lane & 12) * 4);
#pragma unroll
      for (int i = 0; i < 5; ++i) A[i] = u32x2{ln2(xa[i][0], ma, ra), ln2(xa[i][1], ma, ra)};
    }
    SCHED();
    {
      f32x4 D[4];
#pragma unroll
      for (int c = 0; c < 4; ++c) D[c] = f32x4{0.f, 0.f, 0.f, 0.f};
#pragma unroll
      for (int j = 0; j < 5; ++j) {
        const u32x2* w = wq2 + 4 * j;
        D[0] = mfma16(A[j], u32x2{lolo(w[0][0], w[1][0]), lolo(w[2][0], w[3][0])}, D[0]);
        D[1] = mfma16(A[j], u32x2{hihi(w[0][0], w[1][0]), hihi(w[2][0], w[3][0])}, D[1]);
        D[2] = mfma16(A[j], u32x2{lolo(w[0][1], w[1][1]), lolo(w[2][1], w[3][1])}, D[2]);
        D[3] = mfma16(A[j], u32x2{hihi(w[0][1], w[1][1]), hihi(w[2][1], w[3][1])}, D[3]);
      }
      *(f32x4*)(lds + L_PP + hw * 1024 + lane * 16) = f32x4{D[0][0], D[1][0], D[2][0], D[3][0]};
    }
    const unsigned long long T = (((unsigned long long)__builtin_amdgcn_readfirstlane(ep[1]) << 32) |
                                  (unsigned)__builtin_amdgcn_readfirstlane(ep[0])) + 1ull;
    const unsigned tlo = (unsigned)T, thi = (unsigned)(T >> 32), g0 = tlo << 16, g1 = (unsigned)(T >> 16);
    if (lds_arrive(CT + CT_QP, lane) == 3) {
      STAMP(7);
      const f32x4* pp = (const f32x4*)(lds + L_PP) + lane;
      const u32x4 s = __builtin_bit_cast(u32x4, (pp[0] + pp[64]) + (pp[128] + pp[192]));
      const unsigned qo = sub < 3 ? WS_QX + (((grp * 4 + kq) * 2 + half) * DH + sub * 64 + nn * 4) * 8 : OOB;
      pub4(wsr, qo, u32x4{s[0], tlo, s[1], tlo});
      pub4(wsr, qo + 16, u32x4{s[2], tlo, s[3], tlo});
      if (lane == 0) *(u32x2*)(CT + CT_TAG) = u32x2{g0, g1};
      __builtin_amdgcn_s_sleep(Q_W0);
      wait_until<2>(tb + Q_T0);
      fetch_q(wsr, lds, h, 0, tlo, lane);
      STAMP(1);
    }
    BARRIER();
    if (hw > 0) {
      wait_until<8>(tb + Q_T1);
      fetch_q(wsr, lds, h, hw, tlo, lane);
      lds_post(CT + CT_Q, lane);
    }
    if (hw == 0) {
#pragma nounroll
      for (int b = 0; b < NB; ++b) {
        lds_wait<16>(CT + CT_ML, 4 * b + 4);
        lazy(LAZY);
        u32x4 z;
#pragma nounroll
        for (int it = 0; it < SPIN_MAX; ++it) {
          if (PLAIN && it == 0) z = bld<0>(wsr, WS_MX + (b * NH + h) * 1024 + lane * 16, 0);
          else z = bld<SC1>(wsr, WS_MX + (b * NH + h) * 1024 + lane * 16, 0);
          const unsigned bad = (((z[0] ^ g0) | (z[2] ^ g0)) >> 16) | bad2(z, g1);
          if (__builtin_amdgcn_ballot_w64(bad != 0) == 0) break;
          __builtin_amdgcn_s_sleep(48);
          asm volatile("" ::: "memory");
        }
        const float v = __uint_as_float(lolo(z[0], z[2])), o = dpp<XOR1>(v);
        const float mj = lane & 1 ? o : v, lj = lane & 1 ? 0.f : o;
        float Mh = fmaxf(mj, dpp<XOR2>(mj));
        Mh = fmaxf(Mh, dpp<XOR7>(Mh));
        Mh = max_x16_32(fmaxf(Mh, dpp<ROR8>(Mh)));
        const float ej = __builtin_amdgcn_exp2f((mj - Mh) * LOG2E);
        const int wj = __float_as_int(ej / wave_sum(ej * lj));
        const float w0 = __int_as_float(__builtin_amdgcn_readlane(wj, j8 * 8)), w1_ = __int_as_float(__builtin_amdgcn_readlane(wj, j8 * 8 + 2));
        const float w2_ = __int_as_float(__builtin_amdgcn_readlane(wj, j8 * 8 + 4)), w3 = __int_as_float(__builtin_amdgcn_readlane(wj, j8 * 8 + 6));
        const f32x4* oa = (const f32x4*)(lds + L_OA + b * 3072) + (lane < 48 ? lane : 0);
        const unsigned oo = lane < 48 && !((DBG & 1) && b < 3) ? WS_OX + ((b * NH + h) * 8 + j8) * 1536 + lane * 16 : OOB;
        lds_wait<1>(CT + CT_OA + b, 4);
        if (b == 3) STAMP(3);
        const f32x4 r = (oa[0] * w0 + oa[48] * w1_) + (oa[96] * w2_ + oa[144] * w3);
        const unsigned h0 = pkbf(r[0], r[1]), h1 = pkbf(r[2], r[3]);
        pub4(wsr, oo, u32x4{h0, tlo, h1, tlo});
        pub4(wsr, oo + 768, u32x4{pkbf(r[0] - bflo(h0), r[1] - bfhi(h0)), tlo, pkbf(r[2] - bflo(h1), r[3] - bfhi(h1)), tlo});
        lds_post(CT + CT_PO, lane);
      }
    } else if (hw == 1) {
      const rsrc_t wor = mkrs(wo + (size_t)h * DH * (DM * 2) + j8 * 160, NO_WO ? 0 : (DH - 1) * (DM * 2) + 160);
      const unsigned wo1 = kq * (4 * DM * 2) + nn * 8, wo2 = kq * (4 * DM * 2) + 128 + nn * 2;
      u32x4 F[6][5];
#pragma unroll
      for (int kb = 0; kb < 6; ++kb) {
        u32x2 w[8];
        unsigned u[8];
#pragma unroll
        for (int j = 0; j < 8; ++j) {
          wait_until<8>(tb + WO_T0 + (8 * kb + j) * WO_DT);
          LOAD80(w[j], u[j], wor, wo1, wo2, (32 * kb + 16 * (j >> 2) + (j & 3)) * (DM * 2));
          SCHED();
        }
#pragma unroll
        for (int k = 0; k < 2; ++k) {
          F[kb][2 * k] = u32x4{lolo(w[0][k], w[1][k]), lolo(w[2][k], w[3][k]), lolo(w[4][k], w[5][k]), lolo(w[6][k], w[7][k])};
          F[kb][2 * k + 1] = u32x4{hihi(w[0][k], w[1][k]), hihi(w[2][k], w[3][k]), hihi(w[4][k], w[5][k]), hihi(w[6][k], w[7][k])};
        }
        F[kb][4] = u32x4{lolo(u[0], u[1]), lolo(u[2], u[3]), lolo(u[4], u[5]), lolo(u[6], u[7])};
        asm volatile("" : "+v"(F[kb][0]), "+v"(F[kb][1]), "+v"(F[kb][2]), "+v"(F[kb][3]), "+v"(F[kb][4]) :: "memory");
        SCHED();
      }
      lds_wait<8>(CT + CT_YM, 1);
#pragma nounroll
      for (int b = 0; b < NB; ++b) {
        const float* ym = (const float*)(lds + L_YM) + b * 80;
        const float m0 = ym[4 * nn + kq], m1 = ym[64 + nn];
        const unsigned oo = WS_OX + ((b * NH + h) * 16 + nn) * 768 + kq * 16;
        const unsigned yo = ((DBG & 4) && b < 3) ? OOB : WS_Y + ((b * 40 + j8 * 5) * NH + h) * 128;
        lds_wait<1>(CT + CT_PO, b + 1);
        if ((DBG & 3) && b < 3) { lds_post(CT + CT_PY, lane); continue; }
        if (b < 3) lazy(LAZY); else __builtin_amdgcn_s_sleep(P_W0);
        u32x4 z[12];
#pragma nounroll
        for (int it = 0; it < SPIN_MAX; ++it) {
          unsigned bad = 0;
          if (PLAIN && it == 0 && b < 3) {
#pragma unroll
            for (int i = 0; i < 12; ++i) z[i] = bld<0>(wsr, oo + i * 64, 0);
          } else {
#pragma unroll
            for (int i = 0; i < 12; ++i) z[i] = bld<SC1>(wsr, oo + i * 64, 0);
          }
#pragma unroll
          for (int i = 0; i < 12; ++i) bad |= bad2(z[i], tlo);
          if (__builtin_amdgcn_ballot_w64(bad != 0) == 0) break;
          __builtin_amdgcn_s_sleep(P_W1);
          asm volatile("" ::: "memory");
        }
        if (b == 3) STAMP(5);
        f32x4 D[5];
#pragma unroll
        for (int c = 0; c < 5; ++c) D[c] = f32x4{0.f, 0.f, 0.f, 0.f};
#pragma unroll
        for (int kb = 0; kb < 6; ++kb) {
          const u32x4 a = {z[2 * kb][0], z[2 * kb][2], z[2 * kb + 1][0], z[2 * kb + 1][2]};
#pragma unroll
          for (int c = 0; c < 5; ++c) D[c] = mfma(a, F[kb][c], D[c]);
        }
        float t[5];
#pragma unroll
        for (int c = 0; c < 5; ++c) t[c] = (D[c][0] + D[c][1]) + (D[c][2] + D[c][3]);
        const float y0 = rs16(rs32(t[0], t[2]), rs32(t[1], t[3])) + m0;
        t[4] = rs32(t[4], t[4]);
        const float y1 = rs16(t[4], t[4]) + m1;
        pub2(wsr, yo + (nn >> 2) * (NH * 128) + ((nn & 3) * 4 + kq) * 8, u32x2{__float_as_uint(y0), tlo});
        pub2(wsr, lane < 16 ? yo + 4 * (NH * 128) + nn * 8 : OOB, u32x2{__float_as_uint(y1), tlo});
        lds_post(CT + CT_PY, lane);
      }
    } else if (hw == 2) {
      const rsrc_t w2r = mkrs(w2 + (size_t)h * 48 * (DM * 2) + j8 * 160, NO_MLP ? 0 : 47 * (DM * 2) + 160);
      u32x2 w[12], bb[3];
      unsigned u[12];
#pragma unroll
      for (int j = 0; j < 12; ++j) {
        wait_until<8>(tb + MLP_T0 + j * MLP_DT);
        LOAD80(w[j], u[j], w2r, kq * (12 * DM * 2) + nn * 8, kq * (12 * DM * 2) + 128 + nn * 2, j * (DM * 2));
        SCHED();
      }
#pragma unroll
      for (int j = 0; j < 3; ++j) bb[j] = *(const u32x2*)(b1 + h * 48 + kq * 12 + j * 4);
      const unsigned ho = WS_QX + (((32 + (h >> 2)) * 4 + (nn >> 2)) * 2 * DH + (h & 3) * 48 + kq * 12) * 8;
      u32x4 z[12];
#pragma nounroll
      for (int it = 0; it < SPIN_MAX; ++it) {
        unsigned bad = 0;
        if (PLAIN && it == 0) {
#pragma unroll
          for (int i = 0; i < 12; ++i) z[i] = bld<0>(wsr, ho + (i % 6) * 16 + (i / 6) * (DH * 8), 0);
        } else {
#pragma unroll
          for (int i = 0; i < 12; ++i) z[i] = bld<SC1>(wsr, ho + (i % 6) * 16 + (i / 6) * (DH * 8), 0);
        }
#pragma unroll
        for (int i = 0; i < 12; ++i) bad |= bad2(z[i], tlo);
        if (__builtin_amdgcn_ballot_w64(bad != 0) == 0) break;
        __builtin_amdgcn_s_sleep(48);
        asm volatile("" ::: "memory");
      }
      f32x4 D[5];
#pragma unroll
      for (int c = 0; c < 5; ++c) D[c] = f32x4{0.f, 0.f, 0.f, 0.f};
#pragma unroll
      for (int j = 0; j < 3; ++j) {
        u32x2 a;
#pragma unroll
        for (int k = 0; k < 2; ++k) {
          const u32x4 p = z[2 * j + k], q = z[6 + 2 * j + k];
          float h0 = rbf(rbf(__uint_as_float(p[0]) + __uint_as_float(q[0])) + bflo(bb[j][k]));
          float h1 = rbf(rbf(__uint_as_float(p[2]) + __uint_as_float(q[2])) + bfhi(bb[j][k]));
          h0 = h0 > 0.f ? h0 : 0.f; h1 = h1 > 0.f ? h1 : 0.f;
          a[k] = pkbf(h0 * h0, h1 * h1);
        }
        const u32x2* v = w + 4 * j;
        D[0] = mfma16(a, u32x2{lolo(v[0][0], v[1][0]), lolo(v[2][0], v[3][0])}, D[0]);
        D[1] = mfma16(a, u32x2{hihi(v[0][0], v[1][0]), hihi(v[2][0], v[3][0])}, D[1]);
        D[2] = mfma16(a, u32x2{lolo(v[0][1], v[1][1]), lolo(v[2][1], v[3][1])}, D[2]);
        D[3] = mfma16(a, u32x2{hihi(v[0][1], v[1][1]), hihi(v[2][1], v[3][1])}, D[3]);
        D[4] = mfma16(a, u32x2{lolo(u[4 * j], u[4 * j + 1]), lolo(u[4 * j + 2], u[4 * j + 3])}, D[4]);
      }
      float* ym = (float*)(lds + L_YM) + kq * 80;
      *(f32x4*)(ym + 4 * nn) = f32x4{D[0][0], D[1][0], D[2][0], D[3][0]};
      ym[64 + nn] = D[4][0];
      lds_post(CT + CT_YM, lane);
    } else if (bid < 160 && (!(DBG & 15) || bid >= 120)) {
      const int rb = bid / 40, col = (bid % 40) * 16 + (lane & 7) * 2;
      const unsigned xv = *(const unsigned*)(x + rb * DM + col), bv = *(const unsigned*)(b2 + col);
      const float r0 = bflo(xv) + bflo(bv), r1 = bfhi(xv) + bfhi(bv);
      lds_wait<1>(CT + CT_PY, rb + 1);
      if (rb < 3) lazy(LAZY); else __builtin_amdgcn_s_sleep(P_W0);
      u32x4 z[4];
#pragma nounroll
      for (int it = 0; it < SPIN_MAX; ++it) {
#pragma unroll
        for (int i = 0; i < 4; ++i) z[i] = bld<SC1>(wsr, WS_Y + bid * (NH * 128) + lane * 16, i * 1024);
        if (__builtin_amdgcn_ballot_w64((bad2(z[0], tlo) | bad2(z[1], tlo) | bad2(z[2], tlo) | bad2(z[3], tlo)) != 0) == 0) break;
        __builtin_amdgcn_s_sleep(P_W1);
        asm volatile("" ::: "memory");
      }
      STAMP(6);
      float s0 = (__uint_as_float(z[0][0]) + __uint_as_float(z[1][0])) + (__uint_as_float(z[2][0]) + __uint_as_float(z[3][0]));
      float s1 = (__uint_as_float(z[0][2]) + __uint_as_float(z[1][2])) + (__uint_as_float(z[2][2]) + __uint_as_float(z[3][2]));
      s0 += dpp<ROR8>(s0); s1 += dpp<ROR8>(s1);
      s0 = rs16(s0, s0); s1 = rs16(s1, s1);
      s0 = rs32(s0, s0); s1 = rs32(s1, s1);

      if (lane < 8) *(unsigned*)(out + rb * DM + col) = pkbf(r0 + s0, r1 + s1);
      if (bid == ((DBG & 15) ? 120 : 0)) pub2(wsr, lane == 0 ? WS_EP : OOB, u32x2{tlo, thi});
    }
  } else {
    const int sw = wave - 4, g = lane >> 3, c = lane & 7;
    const size_t base = ((size_t)h * SK + (size_t)j8 * (SK / 8)) * (DH * 2);
    constexpr size_t BSTR = (size_t)NH * SK * DH * 2;
    const unsigned loff = g * (DH * 2) + c * 16;
    int tc = sw + (j8 * 32 + h) * ROTMUL;
    const unsigned po = (((bid >> 3) & 15) == 0 && sw == 0 && lane < 4) ? WS_PC + 4096 + ((bid & 7) * 2 + (bid >> 7)) * 256 + lane * 16 : OOB;
    const unsigned ro = lane < 16 ? WS_PC + lane * 256 : OOB;
    int nt = sw * (TAU16 / 8), dmin = -100000, vo = T0OFF * 16, sg = vo, dv = 0, tcu = TAUC16 / 2, n = 2;
    u32x4 R[16][3];
#define ISSUE(j, r, CM) do { if (WIN) asm volatile("s_waitcnt vmcnt(%0)" :: "n"(WIN) : "memory"); vo += dv; const int d_ = max(vo + nt, dmin); const unsigned o_ = (unsigned)(tc & 127) * TILE_B; \
      const int l_ = wait_until<1>(tb + (unsigned)(d_ >> 4)), f_ = loads_in_flight(); SCHED(); \
      CM; \
      _Pragma("unroll") for (int i_ = 0; i_ < 3; ++i_) R[j][i_] = bld<SAUX>(r, loff + i_ * 128, o_); SCHED(); \
      sg = (sg * 3 + (d_ + l_ * 16 - nt + f_ * (TAU16 / 6))) >> 2; nt += TAU16 / 2; dmin = d_ + l_ * 16 + tcu; tc += 4; SCHED(); } while (0)
#define KTILE(j, J, pk) do { \
      float s0_ = 0.f, s1_ = 0.f, s2_ = 0.f; \
      _Pragma("unroll") for (int k_ = 0; k_ < 4; ++k_) { \
        s0_ = dot2(R[j][0][k_], q[0][k_], s0_); s1_ = dot2(R[j][1][k_], q[1][k_], s1_); s2_ = dot2(R[j][2][k_], q[2][k_], s2_); } \
      float s_ = (s0_ + s1_) + s2_; \
      s_ += dpp<XOR1>(s_); s_ += dpp<XOR2>(s_); s_ += dpp<XOR7>(s_); \
      pk = c == (J) ? s_ : pk; \
      asm volatile("" : "+v"(pk) :: "memory"); SCHED(); } while (0)
#define VTILE(j) do { \
      const float p_ = bcast8<j>(S0); \
      _Pragma("unroll") for (int i_ = 0; i_ < 3; ++i_) _Pragma("unroll") for (int k_ = 0; k_ < 4; ++k_) \
        acc[i_ * 4 + k_] = __builtin_elementwise_fma(f32x2{bflo(R[j][i_][k_]), bfhi(R[j][i_][k_])}, f32x2{p_, p_}, acc[i_ * 4 + k_]); \
      asm volatile("" : "+v"(acc[0]), "+v"(acc[1]), "+v"(acc[2]), "+v"(acc[3]), "+v"(acc[4]), "+v"(acc[5]), \
                        "+v"(acc[6]), "+v"(acc[7]), "+v"(acc[8]), "+v"(acc[9]), "+v"(acc[10]), "+v"(acc[11]) :: "memory"); SCHED(); } while (0)
#define TRC() (void)0
#define CONTROL(j) do { \
      if ((j) == C_AT && C_G == 0) TRC(); \
      if ((j) == C_AT && C_G) { \
        const int d_ = (int)(e - tb * 16u) - vo; \
        int y_ = (d_ > -32000 && d_ < 32000) ? d_ : (n > 2 ? C_MISS : 0); \
        y_ = (y_ + dppi<XOR1>(y_)) >> 1; \
        y_ = max(y_, dppi<XOR2>(y_)); y_ = max(y_, dppi<XOR7>(y_)); y_ = max(y_, dppi<ROR8>(y_)); \
        dv = (max(0, min(__builtin_amdgcn_readfirstlane(y_) - C_SLACK, C_CAP)) * C_G) >> 11; TRC(); SCHED(); } } while (0)
#define CTLMEM(j) do { if (C_G == 0) break; \
      if ((j) == 0) e = __builtin_amdgcn_raw_buffer_load_b32(wsr, ro, n * 4096, 0); \
      if ((j) == 2) { const unsigned v_ = tb * 16u + (unsigned)sg; __builtin_amdgcn_raw_buffer_store_b128(u32x4{v_, v_, v_, v_}, wsr, po, n * 4096, SC1); } } while (0)
    {
      const rsrc_t krs = mkrs(kc + base, PART_B);
#pragma unroll
      for (int j = 0; j < 16; ++j) { if (j < PRE) vo -= T0OFF * 16; ISSUE((j + 8) & 15, krs, (void)0); if (j < PRE) vo += T0OFF * 16; }
    }
    STAMP(1);
    BARRIER();
    u32x4 q[3];
#pragma unroll
    for (int i = 0; i < 3; ++i) q[i] = *(const u32x4*)(lds + L_QB + 128 * i + 16 * c);
    const unsigned g0 = __builtin_amdgcn_readfirstlane(CT[CT_TAG]), g1 = __builtin_amdgcn_readfirstlane(CT[CT_TAG + 1]);
    STAMP(2);
    float S0 = 0.f, S1 = 0.f, S2 = 0.f, S3 = 0.f;
    {
      const rsrc_t krs = mkrs(kc + base, PART_B);
#pragma unroll
      for (int j = 0; j < 8; ++j) { KTILE(j, j, S3); ISSUE(j, krs, (void)0); KTILE(8 + j, j, S2); }
    }
    int it = 2;
#pragma nounroll
    for (int b = 0; b < NB; ++b) {
#pragma nounroll
      for (; it < 4; ++it, ++n) {
        const rsrc_t r = mkrs((it < 3 ? kc : vc) + base + b * BSTR, PART_B);
        unsigned e;
        S0 = S1; S1 = S2; S2 = S3;
#pragma unroll
        for (int j = 0; j < 8; ++j) { KTILE(j, j, S3); CONTROL(j); ISSUE(j, r, CTLMEM(j)); }
      }
      {
        S0 = rbf(rbf(S0) * SCALE); S1 = rbf(rbf(S1) * SCALE); S2 = rbf(rbf(S2) * SCALE); S3 = rbf(rbf(S3) * SCALE);
        float m = fmaxf(fmaxf(S0, S1), fmaxf(S2, S3));
        m = fmaxf(m, dpp<XOR1>(m)); m = fmaxf(m, dpp<XOR2>(m)); m = fmaxf(m, dpp<XOR7>(m));
        m = max_x16_32(fmaxf(m, dpp<ROR8>(m)));
        S0 = __builtin_amdgcn_exp2f((S0 - m) * LOG2E); S1 = __builtin_amdgcn_exp2f((S1 - m) * LOG2E);
        S2 = __builtin_amdgcn_exp2f((S2 - m) * LOG2E); S3 = __builtin_amdgcn_exp2f((S3 - m) * LOG2E);
        const float l = wave_sum((S0 + S1) + (S2 + S3));
        const unsigned u = __float_as_uint(lane & 1 ? l : m);
        pub4(wsr, lane < 2 ? WS_MX + (b * NH + h) * 1024 + (j8 * 4 + sw) * 32 + lane * 16 : OOB,
             u32x4{g0 | (u & 0xffffu), g1, g0 | (u >> 16), g1});
        lds_post(CT + CT_ML, lane);
      }
      f32x2 acc[12];
#pragma unroll
      for (int k = 0; k < 12; ++k) acc[k] = f32x2{0.f, 0.f};
#pragma nounroll
      for (it = 0; it < 4; ++it, ++n) {
        const rsrc_t r = mkrs(it < 3 ? vc + base + b * BSTR : kc + base + (b < 3 ? b + 1 : b) * BSTR, n == 31 ? 0 : PART_B);
        if (n == 31) { nt -= 1 << 24; dmin = nt; tcu = -(1 << 24); }
        unsigned e;
        VTILE(0); CONTROL(0); ISSUE(0, r, CTLMEM(0)); VTILE(1); CONTROL(1); ISSUE(1, r, CTLMEM(1));
        VTILE(2); CONTROL(2); ISSUE(2, r, CTLMEM(2)); VTILE(3); CONTROL(3); ISSUE(3, r, CTLMEM(3));
        VTILE(4); CONTROL(4); ISSUE(4, r, CTLMEM(4)); VTILE(5); CONTROL(5); ISSUE(5, r, CTLMEM(5));
        VTILE(6); CONTROL(6); ISSUE(6, r, CTLMEM(6)); VTILE(7); CONTROL(7); ISSUE(7, r, CTLMEM(7));
        S0 = S1; S1 = S2; S2 = S3;
      }
      if (b == 3) STAMP(3);
      float* const OA = (float*)(lds + L_OA + b * 3072) + sw * 192 + 8 * c + g;
#pragma unroll
      for (int i = 0; i < 3; ++i) {
        float u[4], v[2];
#pragma unroll
        for (int k = 0; k < 4; ++k) u[k] = rs32(acc[i * 4 + (k >> 1)][k & 1], acc[i * 4 + 2 + (k >> 1)][k & 1]);
#pragma unroll
        for (int k = 0; k < 2; ++k) v[k] = rs16(u[k], u[k + 2]);
        OA[64 * i] = rs8(v[0], v[1], lane & 8);
      }
      lds_post(CT + CT_OA + b, lane);
      if (b < 3) {
        lds_wait<1>(CT + CT_Q, 3);
#pragma unroll
        for (int i = 0; i < 3; ++i) q[i] = *(const u32x4*)(lds + L_QB + (b + 1) * 384 + 128 * i + 16 * c);
      }
      it = 0;
    }
  }
  STAMP(4);
}

extern "C" void kernel_launch(void* const* d_in, const int* in_sizes, int n_in, void* d_out, int out_size, void* d_ws,
                              size_t ws_size, hipStream_t stream) {
  (void)in_sizes; (void)n_in; (void)out_size; (void)ws_size;
  fused_block<<<NWG, NTHR, 0, stream>>>((const unsigned short*)d_in[0], (const unsigned char*)d_in[1],
                                        (const unsigned char*)d_in[2], (const unsigned char*)d_in[3],
                                        (const unsigned char*)d_in[4], (const unsigned char*)d_in[6],
                                        (const unsigned short*)d_in[7], (const unsigned char*)d_in[8],
                                        (const unsigned short*)d_in[9], (unsigned char*)d_ws, (unsigned short*)d_out);
}
```

```cpp
#define C_G2 0
#define C_CAP 3200
#include <hip/hip_runtime.h>
#include <hip/hip_bf16.h>
#include <stdint.h>

typedef unsigned u32x4 __attribute__((ext_vector_type(4)));
typedef unsigned u32x2 __attribute__((ext_vector_type(2)));
typedef float f32x4 __attribute__((ext_vector_type(4)));
typedef float f32x2 __attribute__((ext_vector_type(2)));
typedef __bf16 bf16x2 __attribute__((ext_vector_type(2)));
typedef __bf16 bf16x8 __attribute__((ext_vector_type(8)));
typedef short s16x4 __attribute__((ext_vector_type(4)));
typedef __amdgpu_buffer_rsrc_t rsrc_t;

constexpr int NB = 4, DM = 640, NH = 32, SK = 8192, DH = 192, FF = 1536;
constexpr int NWG = 256, NTHR = 512;
constexpr int TILE_B = 8 * DH * 2;
constexpr int PART_B = 128 * TILE_B;
#define ROTMUL 21
#define TAU16 1365
#define TAUC16 600
#define T0OFF 200
#define FORGIVE 100000
#define C_SLACK 1100
#define C_G 60
#define C_K 2400
#define C_MISS 4800
#define C_AT 6
#define WO_T0 1200
#define WO_DT 90
#define MLP_T0 1300
#define MLP_DT 100
#define LAZY 600
#define P_W0 8
#define P_W1 6
#define WQ_AUX 0
#define WO_AUX 0
#define Q_W0 24
#define Q_W1 24
#define Q_T0 480
#define WIN 0
#define T0MLP T0OFF
#define NO_MLP 0
#define NO_WO 0
#define WQ_SLP 2
#define PRE 1
#define PLAIN 1
#define DBG 0
#define Q_T1 900
#define SAUX 2
#define OUTAUX 16
#define ENC 0
constexpr int NT = 2, SC1 = 16;
constexpr float LOG2E = 1.4426950408889634f;
constexpr float SCALE = 0.07216878364870322f;
constexpr unsigned OOB = 0x40000000u;
constexpr int SPIN_MAX = 1 << 18;

constexpr unsigned WS_EP = 0;
constexpr unsigned WS_PC = 4096;
constexpr unsigned WS_MX = WS_PC + 33 * 4096;
constexpr unsigned WS_QX = WS_MX + NB * NH * 1024;
constexpr unsigned WS_OX = WS_QX + 40 * NB * 2 * DH * 8;
constexpr unsigned WS_Y = WS_OX + NB * NH * 16 * 768;
constexpr unsigned WS_END = WS_Y + NB * 40 * NH * 128;

constexpr int L_PP = 0;
constexpr int L_QB = L_PP + 4096;
constexpr int L_OA = L_QB + 1536;
constexpr int L_YM = L_OA + 4 * 3072;
constexpr int L_CT = L_YM + 1280;
constexpr int L_END = L_CT + 64;
enum { CT_QP, CT_Q, CT_ML, CT_PO, CT_YM, CT_PY, CT_TAG, CT_OA = 8 };

__device__ __forceinline__ rsrc_t mkrs(const void* p, unsigned bytes) {
  return __builtin_amdgcn_make_buffer_rsrc((void*)p, 0, bytes, 0x00020000);
}
template <int AUX> __device__ __forceinline__ u32x4 bld(rsrc_t r, unsigned voff, unsigned soff) {
  return __builtin_amdgcn_raw_buffer_load_b128(r, voff, soff, AUX);
}
__device__ __forceinline__ void pub4(rsrc_t r, unsigned voff, u32x4 v) { __builtin_amdgcn_raw_buffer_store_b128(v, r, voff, 0, SC1); }
__device__ __forceinline__ void pub2(rsrc_t r, unsigned voff, u32x2 v) { __builtin_amdgcn_raw_buffer_store_b64(v, r, voff, 0, SC1); }
__device__ __forceinline__ float dot2(unsigned a, unsigned b, float c) {
  return __builtin_amdgcn_fdot2_f32_bf16(__builtin_bit_cast(bf16x2, a), __builtin_bit_cast(bf16x2, b), c, false);
}
__device__ __forceinline__ f32x4 mfma(u32x4 a, u32x4 b, f32x4 c) {
  return __builtin_amdgcn_mfma_f32_16x16x32_bf16(__builtin_bit_cast(bf16x8, a), __builtin_bit_cast(bf16x8, b), c, 0, 0, 0);
}
__device__ __forceinline__ f32x4 mfma16(u32x2 a, u32x2 b, f32x4 c) {
  return __builtin_amdgcn_mfma_f32_16x16x16bf16_1k(__builtin_bit_cast(s16x4, a), __builtin_bit_cast(s16x4, b), c, 0, 0, 0);
}
__device__ __forceinline__ float bflo(unsigned v) { return __builtin_bit_cast(float, v << 16); }
__device__ __forceinline__ float bfhi(unsigned v) { return __builtin_bit_cast(float, v & 0xffff0000u); }
__device__ __forceinline__ float rbf(float x) { return (float)(__bf16)x; }
__device__ __forceinline__ unsigned pkbf(float lo, float hi) {
  bf16x2 v = {(__bf16)lo, (__bf16)hi};
  return __builtin_bit_cast(unsigned, v);
}
__device__ __forceinline__ unsigned ln2(unsigned v, float mu, float rs) {
  const unsigned d = pkbf(bflo(v) - mu, bfhi(v) - mu);
  return pkbf(bflo(d) * rs, bfhi(d) * rs);
}
__device__ __forceinline__ unsigned lolo(unsigned a, unsigned b) { return __builtin_amdgcn_perm(b, a, 0x05040100u); }
__device__ __forceinline__ unsigned hihi(unsigned a, unsigned b) { return __builtin_amdgcn_perm(b, a, 0x07060302u); }
template <int CTRL> __device__ __forceinline__ float dpp(float x) {
  return __builtin_bit_cast(float, __builtin_amdgcn_mov_dpp(__builtin_bit_cast(int, x), CTRL, 0xf, 0xf, true));
}
constexpr int XOR1 = 0xB1, XOR2 = 0x4E, XOR7 = 0x141, ROR4 = 0x124, ROR8 = 0x128;
__device__ __forceinline__ float rs32(float a, float b) {
  auto s = __builtin_amdgcn_permlane32_swap(__float_as_uint(a), __float_as_uint(b), false, false);
  return __uint_as_float(s[0]) + __uint_as_float(s[1]);
}
__device__ __forceinline__ float rs16(float a, float b) {
  auto s = __builtin_amdgcn_permlane16_swap(__float_as_uint(a), __float_as_uint(b), false, false);
  return __uint_as_float(s[0]) + __uint_as_float(s[1]);
}
__device__ __forceinline__ float rs8(float a, float b, bool up) {
  const float t1 = a + dpp<ROR8>(a), t2 = b + dpp<ROR8>(b);
  return up ? t2 : t1;
}
__device__ __forceinline__ float max_x16_32(float x) {
  auto s = __builtin_amdgcn_permlane16_swap(__float_as_uint(x), __float_as_uint(x), false, false);
  x = fmaxf(__uint_as_float(s[0]), __uint_as_float(s[1]));
  auto t = __builtin_amdgcn_permlane32_swap(__float_as_uint(x), __float_as_uint(x), false, false);
  return fmaxf(__uint_as_float(t[0]), __uint_as_float(t[1]));
}
__device__ __forceinline__ float sum_q16(float x) {
  x += dpp<ROR4>(x); x += dpp<ROR8>(x);
  x = rs16(x, x);
  return rs32(x, x);
}
__device__ __forceinline__ float wave_sum(float x) {
  x += dpp<XOR1>(x); x += dpp<XOR2>(x);
  return sum_q16(x);
}
template <int SLP> __device__ __forceinline__ int wait_until(unsigned due) {
  int late;
  asm volatile("L_pace_%=:\n\t"
               "s_memrealtime vcc\n\t"
               "s_waitcnt lgkmcnt(0)\n\t"
               "s_sub_i32 vcc_lo, vcc_lo, %1\n\t"
               "s_cmp_lt_i32 vcc_lo, 0\n\t"
               "s_cbranch_scc0 L_done_%=\n\t"
               "s_sleep %2\n\t"
               "s_branch L_pace_%=\n"
               "L_done_%=:\n\t"
               "s_mov_b32 %0, vcc_lo"
               : "=s"(late) : "s"(due), "n"(SLP) : "scc", "vcc", "memory");
  return late;
}
__device__ __forceinline__ int loads_in_flight() {
  unsigned ib;
  asm volatile("s_getreg_b32 %0, hwreg(HW_REG_IB_STS)" : "=s"(ib));
  return (ib & 15) | ((ib >> 30) << 4);
}
template <int CTRL> __device__ __forceinline__ int dppi(int x) { return __builtin_amdgcn_mov_dpp(x, CTRL, 0xf, 0xf, true); }
template <int J> __device__ __forceinline__ float bcast8(float x) {
  const int v = __builtin_bit_cast(int, x);
  const int lo = __builtin_amdgcn_update_dpp(0, v, 0x150 + J, 0xf, 0x3, false);
  return __builtin_bit_cast(float, __builtin_amdgcn_update_dpp(lo, v, 0x158 + J, 0xf, 0xc, false));
}
#define SCHED() __builtin_amdgcn_sched_barrier(0)
#define LDS_FENCE() asm volatile("s_waitcnt lgkmcnt(0)" ::: "memory")
#define DRAIN() asm volatile("s_waitcnt vmcnt(0)" ::: "memory")
#define BARRIER() do { SCHED(); LDS_FENCE(); __builtin_amdgcn_s_barrier(); asm volatile("" ::: "memory"); SCHED(); } while (0)
#define STAMP(i) do {} while (0)

__device__ __forceinline__ unsigned lds_arrive(unsigned* cnt, int lane) {
  LDS_FENCE();
  unsigned old = 0;
  if (lane == 0) old = __hip_atomic_fetch_add(cnt, 1u, __ATOMIC_RELAXED, __HIP_MEMORY_SCOPE_WORKGROUP);
  old = __builtin_amdgcn_readfirstlane(old);
  asm volatile("" ::: "memory");
  return old;
}
__device__ __forceinline__ void lds_post(unsigned* cnt, int lane) {
  LDS_FENCE();
  if (lane == 0) __hip_atomic_fetch_add(cnt, 1u, __ATOMIC_RELAXED, __HIP_MEMORY_SCOPE_WORKGROUP);
  asm volatile("" ::: "memory");
}
template <int SLP> __device__ __forceinline__ void lds_wait(unsigned* cnt, unsigned n) {
  while (__builtin_amdgcn_readfirstlane(__hip_atomic_load(cnt, __ATOMIC_RELAXED, __HIP_MEMORY_SCOPE_WORKGROUP)) < n)
    __builtin_amdgcn_s_sleep(SLP);
  asm volatile("" ::: "memory");
}
__device__ __forceinline__ void lazy(int ticks) { wait_until<8>((unsigned)__builtin_amdgcn_s_memrealtime() + ticks); }
__device__ __forceinline__ unsigned bad2(u32x4 v, unsigned tag) { return (v[1] ^ tag) | (v[3] ^ tag); }
#define LOAD80(w, u, r, vo1, vo2, so) do { w = __builtin_amdgcn_raw_buffer_load_b64(r, vo1, so, WO_AUX); \
    u = __builtin_amdgcn_raw_buffer_load_b16(r, vo2, so, WO_AUX); } while (0)

__device__ __forceinline__ void fetch_q(rsrc_t wsr, unsigned char* lds, int h, int n, unsigned tag, int lane) {
  const unsigned qo = WS_QX + (h * 4 + n) * (2 * DH * 8) + lane * 16, qo2 = qo + 1024 + (lane >> 5) * 1024;
  u32x4 qa, qb, qc;
#pragma nounroll
  for (int it = 0; it < SPIN_MAX; ++it) {
    if (PLAIN && it == 0) { qa = bld<0>(wsr, qo, 0); qb = bld<0>(wsr, qo, DH * 8); qc = bld<0>(wsr, qo2, 0); }
    else { qa = bld<SC1>(wsr, qo, 0); qb = bld<SC1>(wsr, qo, DH * 8); qc = bld<SC1>(wsr, qo2, 0); }
    const unsigned bad = bad2(qa, tag) | bad2(qb, tag) | bad2(qc, tag);
    if (__builtin_amdgcn_ballot_w64(bad != 0) == 0) break;
    __builtin_amdgcn_s_sleep(Q_W1);
    asm volatile("" ::: "memory");
  }
  unsigned* QB = (unsigned*)(lds + L_QB + n * 384);
  QB[lane] = pkbf(__uint_as_float(qa[0]) + __uint_as_float(qb[0]), __uint_as_float(qa[2]) + __uint_as_float(qb[2]));
  const float c0 = __uint_as_float(qc[0]), c1 = __uint_as_float(qc[2]);
  const unsigned t = pkbf(rs32(c0, c0), rs32(c1, c1));
  if (lane < 32) QB[64 + lane] = t;
}
__device__ __forceinline__ float sum_row16(float x) {
  x += dpp<XOR1>(x); x += dpp<XOR2>(x); x += dpp<XOR7>(x);
  return x + dpp<ROR8>(x);
}

__global__ void __launch_bounds__(NTHR, 2)
fused_block(const unsigned short* __restrict__ x, const unsigned char* __restrict__ kc, const unsigned char* __restrict__ vc,
            const unsigned char* __restrict__ wq, const unsigned char* __restrict__ wo, const unsigned char* __restrict__ w1,
            const unsigned short* __restrict__ b1, const unsigned char* __restrict__ w2, const unsigned short* __restrict__ b2,
            unsigned char* ws, unsigned short* out) {
  __shared__ __attribute__((aligned(16))) unsigned char lds[L_END];
  const unsigned long long tb64 = __builtin_amdgcn_s_memrealtime();
  const unsigned tb = (unsigned)tb64;
  const int tid = threadIdx.x, lane = tid & 63, wave = __builtin_amdgcn_readfirstlane(tid >> 6);
  const int bid = blockIdx.x;
  const int h = (bid & 7) * 4 + ((bid >> 3) & 3), j8 = bid >> 5;
  unsigned* const CT = (unsigned*)(lds + L_CT);
  const rsrc_t wsr = mkrs(ws, WS_END);
  if (tid < 16) CT[tid] = 0;
  BARRIER();

  if (wave < 4) {
    const int hw = wave;
    const int kq = lane >> 4, nn = lane & 15;
    const bool mlp = j8 >= 6;
    const int half = mlp ? j8 & 1 : j8 / 3, grp = mlp ? 32 + (h >> 2) : h, sub = mlp ? h & 3 : j8 % 3, qr = half * 320 + hw * 80 + kq * 20;
    const unsigned rstr = mlp ? FF * 2 : NH * DH * 2;
    const rsrc_t xr = mkrs(x, NB * DM * 2);
    u32x4 xs[5];
    u32x2 xa[5], wq2[20];
#pragma unroll
    for (int i = 0; i < 5; ++i) xs[i] = bld<0>(xr, kq * (DM * 2) + nn * 16, i * 256);
    const rsrc_t wqr = mkrs((mlp ? w1 : wq) + (grp & 31) * 384 + sub * 128, sub < 3 ? (DM - 1) * rstr + 128 : 0);
#pragma unroll
    for (int i = 0; i < 5; ++i) xa[i] = __builtin_amdgcn_raw_buffer_load_b64(xr, (nn >> 2) * (DM * 2) + qr * 2, i * 8, 0);
    const u32x2 ep = __builtin_amdgcn_raw_buffer_load_b64(wsr, WS_EP, 0, SC1);
    const unsigned wqo = qr * rstr + nn * 8;
#pragma unroll
    for (int j = 0; j < 20; ++j) { wq2[j] = __builtin_amdgcn_raw_buffer_load_b64(wqr, wqo, j * rstr, WQ_AUX); if (WQ_SLP) { SCHED(); __builtin_amdgcn_s_sleep(WQ_SLP); SCHED(); } }
    SCHED();
    float sm = 0.f;
#pragma unroll
    for (int i = 0; i < 5; ++i)
#pragma unroll
      for (int k = 0; k < 4; ++k) sm = dot2(xs[i][k], 0x3f803f80u, sm);
    const float mean = sum_row16(sm) * (1.f / DM), mub = rbf(mean);
    f32x2 sq = {0.f, 0.f};
#pragma unroll
    for (int i = 0; i < 5; ++i)
#pragma unroll
      for (int k = 0; k < 4; ++k) {
        const f32x2 d = f32x2{bflo(xs[i][k]), bfhi(xs[i][k])} - f32x2{mean, mean};
        sq = __builtin_elementwise_fma(d, d, sq);
      }
    const float sd = rbf(__fsqrt_rn(rbf(rbf(sum_row16(sq[0] + sq[1]) * (1.f / DM)) + 1e-5f)));
    const float rsd = 1.f / sd;
    u32x2 A[5];
    {
      const float ma = __shfl(mub, (lane & 12) * 4), ra = __shfl(rsd, (lane & 12) * 4);
#pragma unroll
      for (int i = 0; i < 5; ++i) A[i] = u32x2{ln2(xa[i][0], ma, ra), ln2(xa[i][1], ma, ra)};
    }
    SCHED();
    {
      f32x4 D[4];
#pragma unroll
      for (int c = 0; c < 4; ++c) D[c] = f32x4{0.f, 0.f, 0.f, 0.f};
#pragma unroll
      for (int j = 0; j < 5; ++j) {
        const u32x2* w = wq2 + 4 * j;
        D[0] = mfma16(A[j], u32x2{lolo(w[0][0], w[1][0]), lolo(w[2][0], w[3][0])}, D[0]);
        D[1] = mfma16(A[j], u32x2{hihi(w[0][0], w[1][0]), hihi(w[2][0], w[3][0])}, D[1]);
        D[2] = mfma16(A[j], u32x2{lolo(w[0][1], w[1][1]), lolo(w[2][1], w[3][1])}, D[2]);
        D[3] = mfma16(A[j], u32x2{hihi(w[0][1], w[1][1]), hihi(w[2][1], w[3][1])}, D[3]);
      }
      *(f32x4*)(lds + L_PP + hw * 1024 + lane * 16) = f32x4{D[0][0], D[1][0], D[2][0], D[3][0]};
    }
    const unsigned long long T = (((unsigned long long)__builtin_amdgcn_readfirstlane(ep[1]) << 32) |
                                  (unsigned)__builtin_amdgcn_readfirstlane(ep[0])) + 1ull;
    const unsigned tlo = (unsigned)T, thi = (unsigned)(T >> 32), g0 = tlo << 16, g1 = (unsigned)(T >> 16);
    if (lds_arrive(CT + CT_QP, lane) == 3) {
      STAMP(7);
      const f32x4* pp = (const f32x4*)(lds + L_PP) + lane;
      const u32x4 s = __builtin_bit_cast(u32x4, (pp[0] + pp[64]) + (pp[128] + pp[192]));
      const unsigned qo = sub < 3 ? WS_QX + (((grp * 4 + kq) * 2 + half) * DH + sub * 64 + nn * 4) * 8 : OOB;
      pub4(wsr, qo, u32x4{s[0], tlo, s[1], tlo});
      pub4(wsr, qo + 16, u32x4{s[2], tlo, s[3], tlo});
      if (lane == 0) *(u32x2*)(CT + CT_TAG) = u32x2{g0, g1};
      __builtin_amdgcn_s_sleep(Q_W0);
      wait_until<2>(tb + Q_T0);
      fetch_q(wsr, lds, h, 0, tlo, lane);
      STAMP(1);
    }
    BARRIER();
    if (hw > 0) {
      wait_until<8>(tb + Q_T1);
      fetch_q(wsr, lds, h, hw, tlo, lane);
      lds_post(CT + CT_Q, lane);
    }
    if (hw == 0) {
#pragma nounroll
      for (int b = 0; b < NB; ++b) {
        lds_wait<16>(CT + CT_ML, 4 * b + 4);
        lazy(LAZY);
        u32x4 z;
#pragma nounroll
        for (int it = 0; it < SPIN_MAX; ++it) {
          if (PLAIN && it == 0) z = bld<0>(wsr, WS_MX + (b * NH + h) * 1024 + lane * 16, 0);
          else z = bld<SC1>(wsr, WS_MX + (b * NH + h) * 1024 + lane * 16, 0);
          const unsigned bad = (((z[0] ^ g0) | (z[2] ^ g0)) >> 16) | bad2(z, g1);
          if (__builtin_amdgcn_ballot_w64(bad != 0) == 0) break;
          __builtin_amdgcn_s_sleep(48);
          asm volatile("" ::: "memory");
        }
        const float v = __uint_as_float(lolo(z[0], z[2])), o = dpp<XOR1>(v);
        const float mj = lane & 1 ? o : v, lj = lane & 1 ? 0.f : o;
        float Mh = fmaxf(mj, dpp<XOR2>(mj));
        Mh = fmaxf(Mh, dpp<XOR7>(Mh));
        Mh = max_x16_32(fmaxf(Mh, dpp<ROR8>(Mh)));
        const float ej = __builtin_amdgcn_exp2f((mj - Mh) * LOG2E);
        const int wj = __float_as_int(ej / wave_sum(ej * lj));
        const float w0 = __int_as_float(__builtin_amdgcn_readlane(wj, j8 * 8)), w1_ = __int_as_float(__builtin_amdgcn_readlane(wj, j8 * 8 + 2));
        const float w2_ = __int_as_float(__builtin_amdgcn_readlane(wj, j8 * 8 + 4)), w3 = __int_as_float(__builtin_amdgcn_readlane(wj, j8 * 8 + 6));
        const f32x4* oa = (const f32x4*)(lds + L_OA + b * 3072) + (lane < 48 ? lane : 0);
        const unsigned oo = lane < 48 && !((DBG & 1) && b < 3) ? WS_OX + ((b * NH + h) * 8 + j8) * 1536 + lane * 16 : OOB;
        lds_wait<1>(CT + CT_OA + b, 4);
        if (b == 3) STAMP(3);
        const f32x4 r = (oa[0] * w0 + oa[48] * w1_) + (oa[96] * w2_ + oa[144] * w3);
        const unsigned h0 = pkbf(r[0], r[1]), h1 = pkbf(r[2], r[3]);
        pub4(wsr, oo, u32x4{h0, tlo, h1, tlo});
        pub4(wsr, oo + 768, u32x4{pkbf(r[0] - bflo(h0), r[1] - bfhi(h0)), tlo, pkbf(r[2] - bflo(h1), r[3] - bfhi(h1)), tlo});
        lds_post(CT + CT_PO, lane);
      }
    } else if (hw == 1) {
      const rsrc_t wor = mkrs(wo + (size_t)h * DH * (DM * 2) + j8 * 160, NO_WO ? 0 : (DH - 1) * (DM * 2) + 160);
      const unsigned wo1 = kq * (4 * DM * 2) + nn * 8, wo2 = kq * (4 * DM * 2) + 128 + nn * 2;
      u32x4 F[6][5];
#pragma unroll
      for (int kb = 0; kb < 6; ++kb) {
        u32x2 w[8];
        unsigned u[8];
#pragma unroll
        for (int j = 0; j < 8; ++j) {
          wait_until<8>(tb + WO_T0 + (8 * kb + j) * WO_DT);
          LOAD80(w[j], u[j], wor, wo1, wo2, (32 * kb + 16 * (j >> 2) + (j & 3)) * (DM * 2));
          SCHED();
        }
#pragma unroll
        for (int k = 0; k < 2; ++k) {
          F[kb][2 * k] = u32x4{lolo(w[0][k], w[1][k]), lolo(w[2][k], w[3][k]), lolo(w[4][k], w[5][k]), lolo(w[6][k], w[7][k])};
          F[kb][2 * k + 1] = u32x4{hihi(w[0][k], w[1][k]), hihi(w[2][k], w[3][k]), hihi(w[4][k], w[5][k]), hihi(w[6][k], w[7][k])};
        }
        F[kb][4] = u32x4{lolo(u[0], u[1]), lolo(u[2], u[3]), lolo(u[4], u[5]), lolo(u[6], u[7])};
        asm volatile("" : "+v"(F[kb][0]), "+v"(F[kb][1]), "+v"(F[kb][2]), "+v"(F[kb][3]), "+v"(F[kb][4]) :: "memory");
        SCHED();
      }
      lds_wait<8>(CT + CT_YM, 1);
#pragma nounroll
      for (int b = 0; b < NB; ++b) {
        const float* ym = (const float*)(lds + L_YM) + b * 80;
        const float m0 = ym[4 * nn + kq], m1 = ym[64 + nn];
        const unsigned oo = WS_OX + ((b * NH + h) * 16 + nn) * 768 + kq * 16;
        const unsigned yo = ((DBG & 4) && b < 3) ? OOB : WS_Y + ((b * 40 + j8 * 5) * NH + h) * 128;
        lds_wait<1>(CT + CT_PO, b + 1);
        if ((DBG & 3) && b < 3) { lds_post(CT + CT_PY, lane); continue; }
        if (b < 3) lazy(LAZY); else __builtin_amdgcn_s_sleep(P_W0);
        u32x4 z[12];
#pragma nounroll
        for (int it = 0; it < SPIN_MAX; ++it) {
          unsigned bad = 0;
          if (PLAIN && it == 0 && b < 3) {
#pragma unroll
            for (int i = 0; i < 12; ++i) z[i] = bld<0>(wsr, oo + i * 64, 0);
          } else {
#pragma unroll
            for (int i = 0; i < 12; ++i) z[i] = bld<SC1>(wsr, oo + i * 64, 0);
          }
#pragma unroll
          for (int i = 0; i < 12; ++i) bad |= bad2(z[i], tlo);
          if (__builtin_amdgcn_ballot_w64(bad != 0) == 0) break;
          __builtin_amdgcn_s_sleep(P_W1);
          asm volatile("" ::: "memory");
        }
        if (b == 3) STAMP(5);
        f32x4 D[5];
#pragma unroll
        for (int c = 0; c < 5; ++c) D[c] = f32x4{0.f, 0.f, 0.f, 0.f};
#pragma unroll
        for (int kb = 0; kb < 6; ++kb) {
          const u32x4 a = {z[2 * kb][0], z[2 * kb][2], z[2 * kb + 1][0], z[2 * kb + 1][2]};
#pragma unroll
          for (int c = 0; c < 5; ++c) D[c] = mfma(a, F[kb][c], D[c]);
        }
        float t[5];
#pragma unroll
        for (int c = 0; c < 5; ++c) t[c] = (D[c][0] + D[c][1]) + (D[c][2] + D[c][3]);
        const float y0 = rs16(rs32(t[0], t[2]), rs32(t[1], t[3])) + m0;
        t[4] = rs32(t[4], t[4]);
        const float y1 = rs16(t[4], t[4]) + m1;
        pub2(wsr, yo + (nn >> 2) * (NH * 128) + ((nn & 3) * 4 + kq) * 8, u32x2{__float_as_uint(y0), tlo});
        pub2(wsr, lane < 16 ? yo + 4 * (NH * 128) + nn * 8 : OOB, u32x2{__float_as_uint(y1), tlo});
        lds_post(CT + CT_PY, lane);
      }
    } else if (hw == 2) {
      const rsrc_t w2r = mkrs(w2 + (size_t)h * 48 * (DM * 2) + j8 * 160, NO_MLP ? 0 : 47 * (DM * 2) + 160);
      u32x2 w[12], bb[3];
      unsigned u[12];
#pragma unroll
      for (int j = 0; j < 12; ++j) {
        wait_until<8>(tb + MLP_T0 + j * MLP_DT);
        LOAD80(w[j], u[j], w2r, kq * (12 * DM * 2) + nn * 8, kq * (12 * DM * 2) + 128 + nn * 2, j * (DM * 2));
        SCHED();
      }
#pragma unroll
      for (int j = 0; j < 3; ++j) bb[j] = *(const u32x2*)(b1 + h * 48 + kq * 12 + j * 4);
      const unsigned ho = WS_QX + (((32 + (h >> 2)) * 4 + (nn >> 2)) * 2 * DH + (h & 3) * 48 + kq * 12) * 8;
      u32x4 z[12];
#pragma nounroll
      for (int it = 0; it < SPIN_MAX; ++it) {
        unsigned bad = 0;
        if (PLAIN && it == 0) {
#pragma unroll
          for (int i = 0; i < 12; ++i) z[i] = bld<0>(wsr, ho + (i % 6) * 16 + (i / 6) * (DH * 8), 0);
        } else {
#pragma unroll
          for (int i = 0; i < 12; ++i) z[i] = bld<SC1>(wsr, ho + (i % 6) * 16 + (i / 6) * (DH * 8), 0);
        }
#pragma unroll
        for (int i = 0; i < 12; ++i) bad |= bad2(z[i], tlo);
        if (__builtin_amdgcn_ballot_w64(bad != 0) == 0) break;
        __builtin_amdgcn_s_sleep(48);
        asm volatile("" ::: "memory");
      }
      f32x4 D[5];
#pragma unroll
      for (int c = 0; c < 5; ++c) D[c] = f32x4{0.f, 0.f, 0.f, 0.f};
#pragma unroll
      for (int j = 0; j < 3; ++j) {
        u32x2 a;
#pragma unroll
        for (int k = 0; k < 2; ++k) {
          const u32x4 p = z[2 * j + k], q = z[6 + 2 * j + k];
          float h0 = rbf(rbf(__uint_as_float(p[0]) + __uint_as_float(q[0])) + bflo(bb[j][k]));
          float h1 = rbf(rbf(__uint_as_float(p[2]) + __uint_as_float(q[2])) + bfhi(bb[j][k]));
          h0 = h0 > 0.f ? h0 : 0.f; h1 = h1 > 0.f ? h1 : 0.f;
          a[k] = pkbf(h0 * h0, h1 * h1);
        }
        const u32x2* v = w + 4 * j;
        D[0] = mfma16(a, u32x2{lolo(v[0][0], v[1][0]), lolo(v[2][0], v[3][0])}, D[0]);
        D[1] = mfma16(a, u32x2{hihi(v[0][0], v[1][0]), hihi(v[2][0], v[3][0])}, D[1]);
        D[2] = mfma16(a, u32x2{lolo(v[0][1], v[1][1]), lolo(v[2][1], v[3][1])}, D[2]);
        D[3] = mfma16(a, u32x2{hihi(v[0][1], v[1][1]), hihi(v[2][1], v[3][1])}, D[3]);
        D[4] = mfma16(a, u32x2{lolo(u[4 * j], u[4 * j + 1]), lolo(u[4 * j + 2], u[4 * j + 3])}, D[4]);
      }
      float* ym = (float*)(lds + L_YM) + kq * 80;
      *(f32x4*)(ym + 4 * nn) = f32x4{D[0][0], D[1][0], D[2][0], D[3][0]};
      ym[64 + nn] = D[4][0];
      lds_post(CT + CT_YM, lane);
    } else if (bid < 160 && (!(DBG & 15) || bid >= 120)) {
      const int rb = bid / 40, col = (bid % 40) * 16 + (lane & 7) * 2;
      const unsigned xv = *(const unsigned*)(x + rb * DM + col), bv = *(const unsigned*)(b2 + col);
      const float r0 = bflo(xv) + bflo(bv), r1 = bfhi(xv) + bfhi(bv);
      lds_wait<1>(CT + CT_PY, rb + 1);
      if (rb < 3) lazy(LAZY); else __builtin_amdgcn_s_sleep(P_W0);
      u32x4 z[4];
#pragma nounroll
      for (int it = 0; it < SPIN_MAX; ++it) {
#pragma unroll
        for (int i = 0; i < 4; ++i) z[i] = bld<SC1>(wsr, WS_Y + bid * (NH * 128) + lane * 16, i * 1024);
        if (__builtin_amdgcn_ballot_w64((bad2(z[0], tlo) | bad2(z[1], tlo) | bad2(z[2], tlo) | bad2(z[3], tlo)) != 0) == 0) break;
        __builtin_amdgcn_s_sleep(P_W1);
        asm volatile("" ::: "memory");
      }
      STAMP(6);
      float s0 = (__uint_as_float(z[0][0]) + __uint_as_float(z[1][0])) + (__uint_as_float(z[2][0]) + __uint_as_float(z[3][0]));
      float s1 = (__uint_as_float(z[0][2]) + __uint_as_float(z[1][2])) + (__uint_as_float(z[2][2]) + __uint_as_float(z[3][2]));
      s0 += dpp<ROR8>(s0); s1 += dpp<ROR8>(s1);
      s0 = rs16(s0, s0); s1 = rs16(s1, s1);
      s0 = rs32(s0, s0); s1 = rs32(s1, s1);

      __builtin_amdgcn_raw_buffer_store_b32(pkbf(r0 + s0, r1 + s1), mkrs(out, NB * DM * 2), lane < 8 ? (rb * DM + col) * 2 : OOB, 0, OUTAUX);
      if (bid == ((DBG & 15) ? 120 : 0)) pub2(wsr, lane == 0 ? WS_EP : OOB, u32x2{tlo, thi});
    }
  } else {
    const int sw = wave - 4, g = lane >> 3, c = lane & 7;
    const size_t base = ((size_t)h * SK + (size_t)j8 * (SK / 8)) * (DH * 2);
    constexpr size_t BSTR = (size_t)NH * SK * DH * 2;
    const unsigned loff = g * (DH * 2) + c * 16;
    int tc = sw + (j8 * 32 + h) * ROTMUL;
    const unsigned po = (((bid >> 3) & 15) == 0 && sw == 0 && lane < 4) ? WS_PC + 4096 + ((bid & 7) * 2 + (bid >> 7)) * 256 + lane * 16 : OOB;
    const unsigned ro = lane < 16 ? WS_PC + lane * 256 : OOB;
    int nt = sw * (TAU16 / 8), dmin = -100000, vo = T0OFF * 16, sg = vo, dv = 0, tcu = TAUC16 / 2, n = 2;
    u32x4 R[16][3];
#define ISSUE(j, r, CM) do { if (WIN) asm volatile("s_waitcnt vmcnt(%0)" :: "n"(WIN) : "memory"); vo += dv; const int d_ = max(vo + nt, dmin); const unsigned o_ = (unsigned)(tc & 127) * TILE_B; \
      const int l_ = wait_until<1>(tb + (unsigned)(d_ >> 4)), f_ = loads_in_flight(); SCHED(); \
      CM; \
      _Pragma("unroll") for (int i_ = 0; i_ < 3; ++i_) R[j][i_] = bld<SAUX>(r, loff + i_ * 128, o_); SCHED(); \
      sg = (sg * 3 + (d_ + l_ * 16 - nt + f_ * (TAU16 / 6))) >> 2; nt += TAU16 / 2; dmin = d_ + l_ * 16 + tcu; tc += 4; SCHED(); } while (0)
#define KTILE(j, J, pk) do { \
      float s0_ = 0.f, s1_ = 0.f, s2_ = 0.f; \
      _Pragma("unroll") for (int k_ = 0; k_ < 4; ++k_) { \
        s0_ = dot2(R[j][0][k_], q[0][k_], s0_); s1_ = dot2(R[j][1][k_], q[1][k_], s1_); s2_ = dot2(R[j][2][k_], q[2][k_], s2_); } \
      float s_ = (s0_ + s1_) + s2_; \
      s_ += dpp<XOR1>(s_); s_ += dpp<XOR2>(s_); s_ += dpp<XOR7>(s_); \
      pk = c == (J) ? s_ : pk; \
      asm volatile("" : "+v"(pk) :: "memory"); SCHED(); } while (0)
#define VTILE(j) do { \
      const float p_ = bcast8<j>(S0); \
      _Pragma("unroll") for (int i_ = 0; i_ < 3; ++i_) _Pragma("unroll") for (int k_ = 0; k_ < 4; ++k_) \
        acc[i_ * 4 + k_] = __builtin_elementwise_fma(f32x2{bflo(R[j][i_][k_]), bfhi(R[j][i_][k_])}, f32x2{p_, p_}, acc[i_ * 4 + k_]); \
      asm volatile("" : "+v"(acc[0]), "+v"(acc[1]), "+v"(acc[2]), "+v"(acc[3]), "+v"(acc[4]), "+v"(acc[5]), \
                        "+v"(acc[6]), "+v"(acc[7]), "+v"(acc[8]), "+v"(acc[9]), "+v"(acc[10]), "+v"(acc[11]) :: "memory"); SCHED(); } while (0)
#define TRC() (void)0
#define CONTROL(j) do { \
      if ((j) == C_AT && C_G == 0) TRC(); \
      if ((j) == C_AT && C_G) { \
        const int d_ = (int)(e - tb * 16u) - vo; \
        int y_ = (d_ > -32000 && d_ < 32000) ? d_ : (n > 2 ? C_MISS : 0); \
        y_ = (y_ + dppi<XOR1>(y_)) >> 1; \
        y_ = max(y_, dppi<XOR2>(y_)); y_ = max(y_, dppi<XOR7>(y_)); y_ = max(y_, dppi<ROR8>(y_)); \
        const int x_ = max(0, min(__builtin_amdgcn_readfirstlane(y_) - C_SLACK, C_CAP)); \
        dv = (x_ * C_G + max(0, x_ - C_K) * C_G2) >> 11; TRC(); SCHED(); } } while (0)
#define CTLMEM(j) do { if (C_G == 0) break; \
      if ((j) == 0) e = __builtin_amdgcn_raw_buffer_load_b32(wsr, ro, n * 4096, 0); \
      if ((j) == 2) { const unsigned v_ = tb * 16u + (unsigned)sg; __builtin_amdgcn_raw_buffer_store_b128(u32x4{v_, v_, v_, v_}, wsr, po, n * 4096, SC1); } } while (0)
    {
      const rsrc_t krs = mkrs(kc + base, PART_B);
#pragma unroll
      for (int j = 0; j < 16; ++j) { if (j < PRE) vo -= T0OFF * 16; ISSUE((j + 8) & 15, krs, (void)0); if (j < PRE) vo += T0OFF * 16; }
    }
    STAMP(1);
    BARRIER();
    int enc = 0;
    if (ENC == 1) enc = (unsigned)__builtin_amdgcn_s_memrealtime() - tb;
    u32x4 q[3];
#pragma unroll
    for (int i = 0; i < 3; ++i) q[i] = *(const u32x4*)(lds + L_QB + 128 * i + 16 * c);
    const unsigned g0 = __builtin_amdgcn_readfirstlane(CT[CT_TAG]), g1 = __builtin_amdgcn_readfirstlane(CT[CT_TAG + 1]);
    STAMP(2);
    float S0 = 0.f, S1 = 0.f, S2 = 0.f, S3 = 0.f;
    {
      const rsrc_t krs = mkrs(kc + base, PART_B);
#pragma unroll
      for (int j = 0; j < 8; ++j) { KTILE(j, j, S3); ISSUE(j, krs, (void)0); KTILE(8 + j, j, S2); }
    }
    int it = 2;
#pragma nounroll
    for (int b = 0; b < NB; ++b) {
#pragma nounroll
      for (; it < 4; ++it, ++n) {
        const rsrc_t r = mkrs((it < 3 ? kc : vc) + base + b * BSTR, PART_B);
        unsigned e;
        S0 = S1; S1 = S2; S2 = S3;
#pragma unroll
        for (int j = 0; j < 8; ++j) { KTILE(j, j, S3); CONTROL(j); ISSUE(j, r, CTLMEM(j)); }
      }
      {
        S0 = rbf(rbf(S0) * SCALE); S1 = rbf(rbf(S1) * SCALE); S2 = rbf(rbf(S2) * SCALE); S3 = rbf(rbf(S3) * SCALE);
        float m = fmaxf(fmaxf(S0, S1), fmaxf(S2, S3));
        m = fmaxf(m, dpp<XOR1>(m)); m = fmaxf(m, dpp<XOR2>(m)); m = fmaxf(m, dpp<XOR7>(m));
        m = max_x16_32(fmaxf(m, dpp<ROR8>(m)));
        S0 = __builtin_amdgcn_exp2f((S0 - m) * LOG2E); S1 = __builtin_amdgcn_exp2f((S1 - m) * LOG2E);
        S2 = __builtin_amdgcn_exp2f((S2 - m) * LOG2E); S3 = __builtin_amdgcn_exp2f((S3 - m) * LOG2E);
        const float l = wave_sum((S0 + S1) + (S2 + S3));
        const unsigned u = __float_as_uint(lane & 1 ? l : m);
        pub4(wsr, lane < 2 ? WS_MX + (b * NH + h) * 1024 + (j8 * 4 + sw) * 32 + lane * 16 : OOB,
             u32x4{g0 | (u & 0xffffu), g1, g0 | (u >> 16), g1});
        lds_post(CT + CT_ML, lane);
      }
      f32x2 acc[12];
#pragma unroll
      for (int k = 0; k < 12; ++k) acc[k] = f32x2{0.f, 0.f};
#pragma nounroll
      for (it = 0; it < 4; ++it, ++n) {
        const rsrc_t r = mkrs(it < 3 ? vc + base + b * BSTR : kc + base + (b < 3 ? b + 1 : b) * BSTR, n == 31 ? 0 : PART_B);
        if (n == 31) { nt -= 1 << 24; dmin = nt; tcu = -(1 << 24); }
        unsigned e;
        VTILE(0); CONTROL(0); ISSUE(0, r, CTLMEM(0)); VTILE(1); CONTROL(1); ISSUE(1, r, CTLMEM(1));
        VTILE(2); CONTROL(2); ISSUE(2, r, CTLMEM(2)); VTILE(3); CONTROL(3); ISSUE(3, r, CTLMEM(3));
        VTILE(4); CONTROL(4); ISSUE(4, r, CTLMEM(4)); VTILE(5); CONTROL(5); ISSUE(5, r, CTLMEM(5));
        VTILE(6); CONTROL(6); ISSUE(6, r, CTLMEM(6)); VTILE(7); CONTROL(7); ISSUE(7, r, CTLMEM(7));
        S0 = S1; S1 = S2; S2 = S3;
      }
      if (b == 3) STAMP(3);
      if (ENC == 2 && b == 0) enc = vo >> 4;
      if (ENC == 3 && b == 3) enc = vo >> 4;
      if (ENC == 4 && b == 3) enc = (unsigned)__builtin_amdgcn_s_memrealtime() - tb - 11000;
      if (ENC == 5 && b == 1) enc = vo >> 4;
      float* const OA = (float*)(lds + L_OA + b * 3072) + sw * 192 + 8 * c + g;
#pragma unroll
      for (int i = 0; i < 3; ++i) {
        float u[4], v[2];
#pragma unroll
        for (int k = 0; k < 4; ++k) u[k] = rs32(acc[i * 4 + (k >> 1)][k & 1], acc[i * 4 + 2 + (k >> 1)][k & 1]);
#pragma unroll
        for (int k = 0; k < 2; ++k) v[k] = rs16(u[k], u[k + 2]);
        OA[64 * i] = rs8(v[0], v[1], lane & 8);
      }
      lds_post(CT + CT_OA + b, lane);
      if (b < 3) {
        lds_wait<1>(CT + CT_Q, 3);
#pragma unroll
        for (int i = 0; i < 3; ++i) q[i] = *(const u32x4*)(lds + L_QB + (b + 1) * 384 + 128 * i + 16 * c);
      }
      it = 0;
    }
    if (ENC) wait_until<1>(tb + 15000 + 10 * enc);
  }
  STAMP(4);
}

extern "C" void kernel_launch(void* const* d_in, const int* in_sizes, int n_in, void* d_out, int out_size, void* d_ws,
                              size_t ws_size, hipStream_t stream) {
  (void)in_sizes; (void)n_in; (void)out_size; (void)ws_size;
  fused_block<<<NWG, NTHR, 0, stream>>>((const unsigned short*)d_in[0], (const unsigned char*)d_in[1],
                                        (const unsigned char*)d_in[2], (const unsigned char*)d_in[3],
                                        (const unsigned char*)d_in[4], (const unsigned char*)d_in[6],
                                        (const unsigned short*)d_in[7], (const unsigned char*)d_in[8],
                                        (const unsigned short*)d_in[9], (unsigned char*)d_ws, (unsigned short*)d_out);
}
```

```cpp
#define PROBE 2
#define PTHR 700
#include <hip/hip_runtime.h>
#include <hip/hip_bf16.h>
#include <stdint.h>

typedef unsigned u32x4 __attribute__((ext_vector_type(4)));
typedef unsigned u32x2 __attribute__((ext_vector_type(2)));
typedef float f32x4 __attribute__((ext_vector_type(4)));
typedef float f32x2 __attribute__((ext_vector_type(2)));
typedef __bf16 bf16x2 __attribute__((ext_vector_type(2)));
typedef __bf16 bf16x8 __attribute__((ext_vector_type(8)));
typedef short s16x4 __attribute__((ext_vector_type(4)));
typedef __amdgpu_buffer_rsrc_t rsrc_t;

constexpr int NB = 4, DM = 640, NH = 32, SK = 8192, DH = 192, FF = 1536;
constexpr int NWG = 256, NTHR = 512;
constexpr int TILE_B = 8 * DH * 2;
constexpr int PART_B = 128 * TILE_B;
#define ROTMUL 21
#define TAU16 1365
#define TAUC16 600
#define T0OFF 200
#define FORGIVE 100000
#define C_SLACK 1100
#define C_G 60
#define C_K 2400
#define C_G2 200
#define C_MISS 4800
#define C_AT 6
#define C_CAP 6400
#define WO_T0 1200
#define WO_DT 90
#define MLP_T0 1300
#define MLP_DT 100
#define LAZY 600
#define P_W0 8
#define P_W1 6
#define WQ_AUX 0
#define WO_AUX 0
#define Q_W0 24
#define Q_W1 24
#define Q_T0 480
#define WIN 0
#define T0MLP T0OFF
#define NO_MLP 0
#define NO_WO 0
#define WQ_SLP 2
#define PRE 1
#define PLAIN 1
#define DBG 0
#define Q_T1 900
#define SAUX 2
#define OUTAUX 16
#define ENC 0
#define PLO 0
#define PHI 64
constexpr int NT = 2, SC1 = 16;
constexpr float LOG2E = 1.4426950408889634f;
constexpr float SCALE = 0.07216878364870322f;
constexpr unsigned OOB = 0x40000000u;
constexpr int SPIN_MAX = 1 << 18;

constexpr unsigned WS_EP = 0;
constexpr unsigned WS_PC = 4096;
constexpr unsigned WS_MX = WS_PC + 33 * 4096;
constexpr unsigned WS_QX = WS_MX + NB * NH * 1024;
constexpr unsigned WS_OX = WS_QX + 40 * NB * 2 * DH * 8;
constexpr unsigned WS_Y = WS_OX + NB * NH * 16 * 768;
constexpr unsigned WS_END = WS_Y + NB * 40 * NH * 128;

constexpr int L_PP = 0;
constexpr int L_QB = L_PP + 4096;
constexpr int L_OA = L_QB + 1536;
constexpr int L_YM = L_OA + 4 * 3072;
constexpr int L_CT = L_YM + 1280;
constexpr int L_END = L_CT + 64;
enum { CT_QP, CT_Q, CT_ML, CT_PO, CT_YM, CT_PY, CT_TAG, CT_OA = 8 };

__device__ __forceinline__ rsrc_t mkrs(const void* p, unsigned bytes) {
  return __builtin_amdgcn_make_buffer_rsrc((void*)p, 0, bytes, 0x00020000);
}
template <int AUX> __device__ __forceinline__ u32x4 bld(rsrc_t r, unsigned voff, unsigned soff) {
  return __builtin_amdgcn_raw_buffer_load_b128(r, voff, soff, AUX);
}
__device__ __forceinline__ void pub4(rsrc_t r, unsigned voff, u32x4 v) { __builtin_amdgcn_raw_buffer_store_b128(v, r, voff, 0, SC1); }
__device__ __forceinline__ void pub2(rsrc_t r, unsigned voff, u32x2 v) { __builtin_amdgcn_raw_buffer_store_b64(v, r, voff, 0, SC1); }
__device__ __forceinline__ float dot2(unsigned a, unsigned b, float c) {
  return __builtin_amdgcn_fdot2_f32_bf16(__builtin_bit_cast(bf16x2, a), __builtin_bit_cast(bf16x2, b), c, false);
}
__device__ __forceinline__ f32x4 mfma(u32x4 a, u32x4 b, f32x4 c) {
  return __builtin_amdgcn_mfma_f32_16x16x32_bf16(__builtin_bit_cast(bf16x8, a), __builtin_bit_cast(bf16x8, b), c, 0, 0, 0);
}
__device__ __forceinline__ f32x4 mfma16(u32x2 a, u32x2 b, f32x4 c) {
  return __builtin_amdgcn_mfma_f32_16x16x16bf16_1k(__builtin_bit_cast(s16x4, a), __builtin_bit_cast(s16x4, b), c, 0, 0, 0);
}
__device__ __forceinline__ float bflo(unsigned v) { return __builtin_bit_cast(float, v << 16); }
__device__ __forceinline__ float bfhi(unsigned v) { return __builtin_bit_cast(float, v & 0xffff0000u); }
__device__ __forceinline__ float rbf(float x) { return (float)(__bf16)x; }
__device__ __forceinline__ unsigned pkbf(float lo, float hi) {
  bf16x2 v = {(__bf16)lo, (__bf16)hi};
  return __builtin_bit_cast(unsigned, v);
}
__device__ __forceinline__ unsigned ln2(unsigned v, float mu, float rs) {
  const unsigned d = pkbf(bflo(v) - mu, bfhi(v) - mu);
  return pkbf(bflo(d) * rs, bfhi(d) * rs);
}
__device__ __forceinline__ unsigned lolo(unsigned a, unsigned b) { return __builtin_amdgcn_perm(b, a, 0x05040100u); }
__device__ __forceinline__ unsigned hihi(unsigned a, unsigned b) { return __builtin_amdgcn_perm(b, a, 0x07060302u); }
template <int CTRL> __device__ __forceinline__ float dpp(float x) {
  return __builtin_bit_cast(float, __builtin_amdgcn_mov_dpp(__builtin_bit_cast(int, x), CTRL, 0xf, 0xf, true));
}
constexpr int XOR1 = 0xB1, XOR2 = 0x4E, XOR7 = 0x141, ROR4 = 0x124, ROR8 = 0x128;
__device__ __forceinline__ float rs32(float a, float b) {
  auto s = __builtin_amdgcn_permlane32_swap(__float_as_uint(a), __float_as_uint(b), false, false);
  return __uint_as_float(s[0]) + __uint_as_float(s[1]);
}
__device__ __forceinline__ float rs16(float a, float b) {
  auto s = __builtin_amdgcn_permlane16_swap(__float_as_uint(a), __float_as_uint(b), false, false);
  return __uint_as_float(s[0]) + __uint_as_float(s[1]);
}
__device__ __forceinline__ float rs8(float a, float b, bool up) {
  const float t1 = a + dpp<ROR8>(a), t2 = b + dpp<ROR8>(b);
  return up ? t2 : t1;
}
__device__ __forceinline__ float max_x16_32(float x) {
  auto s = __builtin_amdgcn_permlane16_swap(__float_as_uint(x), __float_as_uint(x), false, false);
  x = fmaxf(__uint_as_float(s[0]), __uint_as_float(s[1]));
  auto t = __builtin_amdgcn_permlane32_swap(__float_as_uint(x), __float_as_uint(x), false, false);
  return fmaxf(__uint_as_float(t[0]), __uint_as_float(t[1]));
}
__device__ __forceinline__ float sum_q16(float x) {
  x += dpp<ROR4>(x); x += dpp<ROR8>(x);
  x = rs16(x, x);
  return rs32(x, x);
}
__device__ __forceinline__ float wave_sum(float x) {
  x += dpp<XOR1>(x); x += dpp<XOR2>(x);
  return sum_q16(x);
}
template <int SLP> __device__ __forceinline__ int wait_until(unsigned due) {
  int late;
  asm volatile("L_pace_%=:\n\t"
               "s_memrealtime vcc\n\t"
               "s_waitcnt lgkmcnt(0)\n\t"
               "s_sub_i32 vcc_lo, vcc_lo, %1\n\t"
               "s_cmp_lt_i32 vcc_lo, 0\n\t"
               "s_cbranch_scc0 L_done_%=\n\t"
               "s_sleep %2\n\t"
               "s_branch L_pace_%=\n"
               "L_done_%=:\n\t"
               "s_mov_b32 %0, vcc_lo"
               : "=s"(late) : "s"(due), "n"(SLP) : "scc", "vcc", "memory");
  return late;
}
__device__ __forceinline__ int loads_in_flight() {
  unsigned ib;
  asm volatile("s_getreg_b32 %0, hwreg(HW_REG_IB_STS)" : "=s"(ib));
  return (ib & 15) | ((ib >> 30) << 4);
}
template <int CTRL> __device__ __forceinline__ int dppi(int x) { return __builtin_amdgcn_mov_dpp(x, CTRL, 0xf, 0xf, true); }
template <int J> __device__ __forceinline__ float bcast8(float x) {
  const int v = __builtin_bit_cast(int, x);
  const int lo = __builtin_amdgcn_update_dpp(0, v, 0x150 + J, 0xf, 0x3, false);
  return __builtin_bit_cast(float, __builtin_amdgcn_update_dpp(lo, v, 0x158 + J, 0xf, 0xc, false));
}
#define SCHED() __builtin_amdgcn_sched_barrier(0)
#define LDS_FENCE() asm volatile("s_waitcnt lgkmcnt(0)" ::: "memory")
#define DRAIN() asm volatile("s_waitcnt vmcnt(0)" ::: "memory")
#define BARRIER() do { SCHED(); LDS_FENCE(); __builtin_amdgcn_s_barrier(); asm volatile("" ::: "memory"); SCHED(); } while (0)
#define STAMP(i) do {} while (0)

__device__ __forceinline__ unsigned lds_arrive(unsigned* cnt, int lane) {
  LDS_FENCE();
  unsigned old = 0;
  if (lane == 0) old = __hip_atomic_fetch_add(cnt, 1u, __ATOMIC_RELAXED, __HIP_MEMORY_SCOPE_WORKGROUP);
  old = __builtin_amdgcn_readfirstlane(old);
  asm volatile("" ::: "memory");
  return old;
}
__device__ __forceinline__ void lds_post(unsigned* cnt, int lane) {
  LDS_FENCE();
  if (lane == 0) __hip_atomic_fetch_add(cnt, 1u, __ATOMIC_RELAXED, __HIP_MEMORY_SCOPE_WORKGROUP);
  asm volatile("" ::: "memory");
}
template <int SLP> __device__ __forceinline__ void lds_wait(unsigned* cnt, unsigned n) {
  while (__builtin_amdgcn_readfirstlane(__hip_atomic_load(cnt, __ATOMIC_RELAXED, __HIP_MEMORY_SCOPE_WORKGROUP)) < n)
    __builtin_amdgcn_s_sleep(SLP);
  asm volatile("" ::: "memory");
}
__device__ __forceinline__ void lazy(int ticks) { wait_until<8>((unsigned)__builtin_amdgcn_s_memrealtime() + ticks); }
__device__ __forceinline__ unsigned bad2(u32x4 v, unsigned tag) { return (v[1] ^ tag) | (v[3] ^ tag); }
#define LOAD80(w, u, r, vo1, vo2, so) do { w = __builtin_amdgcn_raw_buffer_load_b64(r, vo1, so, WO_AUX); \
    u = __builtin_amdgcn_raw_buffer_load_b16(r, vo2, so, WO_AUX); } while (0)

__device__ __forceinline__ void fetch_q(rsrc_t wsr, unsigned char* lds, int h, int n, unsigned tag, int lane) {
  const unsigned qo = WS_QX + (h * 4 + n) * (2 * DH * 8) + lane * 16, qo2 = qo + 1024 + (lane >> 5) * 1024;
  u32x4 qa, qb, qc;
#pragma nounroll
  for (int it = 0; it < SPIN_MAX; ++it) {
    if (PLAIN && it == 0) { qa = bld<0>(wsr, qo, 0); qb = bld<0>(wsr, qo, DH * 8); qc = bld<0>(wsr, qo2, 0); }
    else { qa = bld<SC1>(wsr, qo, 0); qb = bld<SC1>(wsr, qo, DH * 8); qc = bld<SC1>(wsr, qo2, 0); }
    const unsigned bad = bad2(qa, tag) | bad2(qb, tag) | bad2(qc, tag);
    if (__builtin_amdgcn_ballot_w64(bad != 0) == 0) break;
    __builtin_amdgcn_s_sleep(Q_W1);
    asm volatile("" ::: "memory");
  }
  unsigned* QB = (unsigned*)(lds + L_QB + n * 384);
  QB[lane] = pkbf(__uint_as_float(qa[0]) + __uint_as_float(qb[0]), __uint_as_float(qa[2]) + __uint_as_float(qb[2]));
  const float c0 = __uint_as_float(qc[0]), c1 = __uint_as_float(qc[2]);
  const unsigned t = pkbf(rs32(c0, c0), rs32(c1, c1));
  if (lane < 32) QB[64 + lane] = t;
}
__device__ __forceinline__ float sum_row16(float x) {
  x += dpp<XOR1>(x); x += dpp<XOR2>(x); x += dpp<XOR7>(x);
  return x + dpp<ROR8>(x);
}

__global__ void __launch_bounds__(NTHR, 2)
fused_block(const unsigned short* __restrict__ x, const unsigned char* __restrict__ kc, const unsigned char* __restrict__ vc,
            const unsigned char* __restrict__ wq, const unsigned char* __restrict__ wo, const unsigned char* __restrict__ w1,
            const unsigned short* __restrict__ b1, const unsigned char* __restrict__ w2, const unsigned short* __restrict__ b2,
            unsigned char* ws, unsigned short* out) {
  __shared__ __attribute__((aligned(16))) unsigned char lds[L_END];
  const unsigned long long tp64 = __builtin_amdgcn_s_memrealtime();
  const unsigned tp = (unsigned)tp64;
  if (threadIdx.x < 64) {
    const size_t STEP = PROBE == 0 ? (size_t)6291456 : PROBE <= 2 ? (size_t)1572864 : (size_t)30720;
    const unsigned char* pb = (PROBE == 0 ? ws : PROBE == 1 ? kc : PROBE == 2 ? vc : PROBE == 3 ? wq : wo) + blockIdx.x * STEP + (PROBE == 0 ? 4096 * 1024 : 0);
    const rsrc_t pr = mkrs(pb, 16384);
    int lat = 0;
    wait_until<1>(tp + (blockIdx.x >> 3) * 4);
    unsigned a = __builtin_amdgcn_raw_buffer_load_b32(pr, 0, 0, 0);
    asm volatile("s_waitcnt vmcnt(0)" : "+v"(a) :: "memory");
    wait_until<1>(tp + 500 + (blockIdx.x >> 3) * 4);
    for (int i = 1; i <= 4; ++i) {
      const unsigned long long c0 = __builtin_amdgcn_s_memtime();
      asm volatile("s_waitcnt lgkmcnt(0)" ::: "memory");
      unsigned a2 = __builtin_amdgcn_raw_buffer_load_b32(pr, i * 768 + 256, 0, 0);
      asm volatile("s_waitcnt vmcnt(0)" : "+v"(a2) :: "memory");
      const unsigned long long c1 = __builtin_amdgcn_s_memtime();
      lat += (int)(c1 - c0) < PTHR ? 1 : 0;
    }
    __builtin_amdgcn_raw_buffer_store_b32((tp & ~7u) + lat, mkrs(ws + (32 << 20), 1024), threadIdx.x == 0 ? blockIdx.x * 4 : OOB, 0, SC1);
  }
  wait_until<1>(tp + 1200);
  const unsigned long long tb64 = __builtin_amdgcn_s_memrealtime();
  const unsigned tb = (unsigned)tb64;
  const int tid = threadIdx.x, lane = tid & 63, wave = __builtin_amdgcn_readfirstlane(tid >> 6);
  const int bid = blockIdx.x;
  const int h = (bid & 7) * 4 + ((bid >> 3) & 3), j8 = bid >> 5;
  unsigned* const CT = (unsigned*)(lds + L_CT);
  const rsrc_t wsr = mkrs(ws, WS_END);
  if (tid < 16) CT[tid] = 0;
  BARRIER();

  if (wave < 4) {
    const int hw = wave;
    const int kq = lane >> 4, nn = lane & 15;
    const bool mlp = j8 >= 6;
    const int half = mlp ? j8 & 1 : j8 / 3, grp = mlp ? 32 + (h >> 2) : h, sub = mlp ? h & 3 : j8 % 3, qr = half * 320 + hw * 80 + kq * 20;
    const unsigned rstr = mlp ? FF * 2 : NH * DH * 2;
    const rsrc_t xr = mkrs(x, NB * DM * 2);
    u32x4 xs[5];
    u32x2 xa[5], wq2[20];
#pragma unroll
    for (int i = 0; i < 5; ++i) xs[i] = bld<0>(xr, kq * (DM * 2) + nn * 16, i * 256);
    const rsrc_t wqr = mkrs((mlp ? w1 : wq) + (grp & 31) * 384 + sub * 128, sub < 3 ? (DM - 1) * rstr + 128 : 0);
#pragma unroll
    for (int i = 0; i < 5; ++i) xa[i] = __builtin_amdgcn_raw_buffer_load_b64(xr, (nn >> 2) * (DM * 2) + qr * 2, i * 8, 0);
    const u32x2 ep = __builtin_amdgcn_raw_buffer_load_b64(wsr, WS_EP, 0, SC1);
    const unsigned wqo = qr * rstr + nn * 8;
#pragma unroll
    for (int j = 0; j < 20; ++j) { wq2[j] = __builtin_amdgcn_raw_buffer_load_b64(wqr, wqo, j * rstr, WQ_AUX); if (WQ_SLP) { SCHED(); __builtin_amdgcn_s_sleep(WQ_SLP); SCHED(); } }
    SCHED();
    float sm = 0.f;
#pragma unroll
    for (int i = 0; i < 5; ++i)
#pragma unroll
      for (int k = 0; k < 4; ++k) sm = dot2(xs[i][k], 0x3f803f80u, sm);
    const float mean = sum_row16(sm) * (1.f / DM), mub = rbf(mean);
    f32x2 sq = {0.f, 0.f};
#pragma unroll
    for (int i = 0; i < 5; ++i)
#pragma unroll
      for (int k = 0; k < 4; ++k) {
        const f32x2 d = f32x2{bflo(xs[i][k]), bfhi(xs[i][k])} - f32x2{mean, mean};
        sq = __builtin_elementwise_fma(d, d, sq);
      }
    const float sd = rbf(__fsqrt_rn(rbf(rbf(sum_row16(sq[0] + sq[1]) * (1.f / DM)) + 1e-5f)));
    const float rsd = 1.f / sd;
    u32x2 A[5];
    {
      const float ma = __shfl(mub, (lane & 12) * 4), ra = __shfl(rsd, (lane & 12) * 4);
#pragma unroll
      for (int i = 0; i < 5; ++i) A[i] = u32x2{ln2(xa[i][0], ma, ra), ln2(xa[i][1], ma, ra)};
    }
    SCHED();
    {
      f32x4 D[4];
#pragma unroll
      for (int c = 0; c < 4; ++c) D[c] = f32x4{0.f, 0.f, 0.f, 0.f};
#pragma unroll
      for (int j = 0; j < 5; ++j) {
        const u32x2* w = wq2 + 4 * j;
        D[0] = mfma16(A[j], u32x2{lolo(w[0][0], w[1][0]), lolo(w[2][0], w[3][0])}, D[0]);
        D[1] = mfma16(A[j], u32x2{hihi(w[0][0], w[1][0]), hihi(w[2][0], w[3][0])}, D[1]);
        D[2] = mfma16(A[j], u32x2{lolo(w[0][1], w[1][1]), lolo(w[2][1], w[3][1])}, D[2]);
        D[3] = mfma16(A[j], u32x2{hihi(w[0][1], w[1][1]), hihi(w[2][1], w[3][1])}, D[3]);
      }
      *(f32x4*)(lds + L_PP + hw * 1024 + lane * 16) = f32x4{D[0][0], D[1][0], D[2][0], D[3][0]};
    }
    const unsigned long long T = (((unsigned long long)__builtin_amdgcn_readfirstlane(ep[1]) << 32) |
                                  (unsigned)__builtin_amdgcn_readfirstlane(ep[0])) + 1ull;
    const unsigned tlo = (unsigned)T, thi = (unsigned)(T >> 32), g0 = tlo << 16, g1 = (unsigned)(T >> 16);
    if (lds_arrive(CT + CT_QP, lane) == 3) {
      STAMP(7);
      const f32x4* pp = (const f32x4*)(lds + L_PP) + lane;
      const u32x4 s = __builtin_bit_cast(u32x4, (pp[0] + pp[64]) + (pp[128] + pp[192]));
      const unsigned qo = sub < 3 ? WS_QX + (((grp * 4 + kq) * 2 + half) * DH + sub * 64 + nn * 4) * 8 : OOB;
      pub4(wsr, qo, u32x4{s[0], tlo, s[1], tlo});
      pub4(wsr, qo + 16, u32x4{s[2], tlo, s[3], tlo});
      if (lane == 0) *(u32x2*)(CT + CT_TAG) = u32x2{g0, g1};
      __builtin_amdgcn_s_sleep(Q_W0);
      wait_until<2>(tb + Q_T0);
      fetch_q(wsr, lds, h, 0, tlo, lane);
      STAMP(1);
    }
    BARRIER();
    if (hw > 0) {
      wait_until<8>(tb + Q_T1);
      fetch_q(wsr, lds, h, hw, tlo, lane);
      lds_post(CT + CT_Q, lane);
    }
    if (hw == 0) {
#pragma nounroll
      for (int b = 0; b < NB; ++b) {
        lds_wait<16>(CT + CT_ML, 4 * b + 4);
        lazy(LAZY);
        u32x4 z;
#pragma nounroll
        for (int it = 0; it < SPIN_MAX; ++it) {
          if (PLAIN && it == 0) z = bld<0>(wsr, WS_MX + (b * NH + h) * 1024 + lane * 16, 0);
          else z = bld<SC1>(wsr, WS_MX + (b * NH + h) * 1024 + lane * 16, 0);
          const unsigned bad = (((z[0] ^ g0) | (z[2] ^ g0)) >> 16) | bad2(z, g1);
          if (__builtin_amdgcn_ballot_w64(bad != 0) == 0) break;
          __builtin_amdgcn_s_sleep(48);
          asm volatile("" ::: "memory");
        }
        const float v = __uint_as_float(lolo(z[0], z[2])), o = dpp<XOR1>(v);
        const float mj = lane & 1 ? o : v, lj = lane & 1 ? 0.f : o;
        float Mh = fmaxf(mj, dpp<XOR2>(mj));
        Mh = fmaxf(Mh, dpp<XOR7>(Mh));
        Mh = max_x16_32(fmaxf(Mh, dpp<ROR8>(Mh)));
        const float ej = __builtin_amdgcn_exp2f((mj - Mh) * LOG2E);
        const int wj = __float_as_int(ej / wave_sum(ej * lj));
        const float w0 = __int_as_float(__builtin_amdgcn_readlane(wj, j8 * 8)), w1_ = __int_as_float(__builtin_amdgcn_readlane(wj, j8 * 8 + 2));
        const float w2_ = __int_as_float(__builtin_amdgcn_readlane(wj, j8 * 8 + 4)), w3 = __int_as_float(__builtin_amdgcn_readlane(wj, j8 * 8 + 6));
        const f32x4* oa = (const f32x4*)(lds + L_OA + b * 3072) + (lane < 48 ? lane : 0);
        const unsigned oo = lane < 48 && !((DBG & 1) && b < 3) ? WS_OX + ((b * NH + h) * 8 + j8) * 1536 + lane * 16 : OOB;
        lds_wait<1>(CT + CT_OA + b, 4);
        if (b == 3) STAMP(3);
        const f32x4 r = (oa[0] * w0 + oa[48] * w1_) + (oa[96] * w2_ + oa[144] * w3);
        const unsigned h0 = pkbf(r[0], r[1]), h1 = pkbf(r[2], r[3]);
        pub4(wsr, oo, u32x4{h0, tlo, h1, tlo});
        pub4(wsr, oo + 768, u32x4{pkbf(r[0] - bflo(h0), r[1] - bfhi(h0)), tlo, pkbf(r[2] - bflo(h1), r[3] - bfhi(h1)), tlo});
        lds_post(CT + CT_PO, lane);
      }
    } else if (hw == 1) {
      const rsrc_t wor = mkrs(wo + (size_t)h * DH * (DM * 2) + j8 * 160, NO_WO ? 0 : (DH - 1) * (DM * 2) + 160);
      const unsigned wo1 = kq * (4 * DM * 2) + nn * 8, wo2 = kq * (4 * DM * 2) + 128 + nn * 2;
      u32x4 F[6][5];
#pragma unroll
      for (int kb = 0; kb < 6; ++kb) {
        u32x2 w[8];
        unsigned u[8];
#pragma unroll
        for (int j = 0; j < 8; ++j) {
          wait_until<8>(tb + WO_T0 + (8 * kb + j) * WO_DT);
          LOAD80(w[j], u[j], wor, wo1, wo2, (32 * kb + 16 * (j >> 2) + (j & 3)) * (DM * 2));
          SCHED();
        }
#pragma unroll
        for (int k = 0; k < 2; ++k) {
          F[kb][2 * k] = u32x4{lolo(w[0][k], w[1][k]), lolo(w[2][k], w[3][k]), lolo(w[4][k], w[5][k]), lolo(w[6][k], w[7][k])};
          F[kb][2 * k + 1] = u32x4{hihi(w[0][k], w[1][k]), hihi(w[2][k], w[3][k]), hihi(w[4][k], w[5][k]), hihi(w[6][k], w[7][k])};
        }
        F[kb][4] = u32x4{lolo(u[0], u[1]), lolo(u[2], u[3]), lolo(u[4], u[5]), lolo(u[6], u[7])};
        asm volatile("" : "+v"(F[kb][0]), "+v"(F[kb][1]), "+v"(F[kb][2]), "+v"(F[kb][3]), "+v"(F[kb][4]) :: "memory");
        SCHED();
      }
      lds_wait<8>(CT + CT_YM, 1);
#pragma nounroll
      for (int b = 0; b < NB; ++b) {
        const float* ym = (const float*)(lds + L_YM) + b * 80;
        const float m0 = ym[4 * nn + kq], m1 = ym[64 + nn];
        const unsigned oo = WS_OX + ((b * NH + h) * 16 + nn) * 768 + kq * 16;
        const unsigned yo = ((DBG & 4) && b < 3) ? OOB : WS_Y + ((b * 40 + j8 * 5) * NH + h) * 128;
        lds_wait<1>(CT + CT_PO, b + 1);
        if ((DBG & 3) && b < 3) { lds_post(CT + CT_PY, lane); continue; }
        if (b < 3) lazy(LAZY); else __builtin_amdgcn_s_sleep(P_W0);
        u32x4 z[12];
#pragma nounroll
        for (int it = 0; it < SPIN_MAX; ++it) {
          unsigned bad = 0;
          if (PLAIN && it == 0 && b < 3) {
#pragma unroll
            for (int i = 0; i < 12; ++i) z[i] = bld<0>(wsr, oo + i * 64, 0);
          } else {
#pragma unroll
            for (int i = 0; i < 12; ++i) z[i] = bld<SC1>(wsr, oo + i * 64, 0);
          }
#pragma unroll
          for (int i = 0; i < 12; ++i) bad |= bad2(z[i], tlo);
          if (__builtin_amdgcn_ballot_w64(bad != 0) == 0) break;
          __builtin_amdgcn_s_sleep(P_W1);
          asm volatile("" ::: "memory");
        }
        if (b == 3) STAMP(5);
        f32x4 D[5];
#pragma unroll
        for (int c = 0; c < 5; ++c) D[c] = f32x4{0.f, 0.f, 0.f, 0.f};
#pragma unroll
        for (int kb = 0; kb < 6; ++kb) {
          const u32x4 a = {z[2 * kb][0], z[2 * kb][2], z[2 * kb + 1][0], z[2 * kb + 1][2]};
#pragma unroll
          for (int c = 0; c < 5; ++c) D[c] = mfma(a, F[kb][c], D[c]);
        }
        float t[5];
#pragma unroll
        for (int c = 0; c < 5; ++c) t[c] = (D[c][0] + D[c][1]) + (D[c][2] + D[c][3]);
        const float y0 = rs16(rs32(t[0], t[2]), rs32(t[1], t[3])) + m0;
        t[4] = rs32(t[4], t[4]);
        const float y1 = rs16(t[4], t[4]) + m1;
        pub2(wsr, yo + (nn >> 2) * (NH * 128) + ((nn & 3) * 4 + kq) * 8, u32x2{__float_as_uint(y0), tlo});
        pub2(wsr, lane < 16 ? yo + 4 * (NH * 128) + nn * 8 : OOB, u32x2{__float_as_uint(y1), tlo});
        lds_post(CT + CT_PY, lane);
      }
    } else if (hw == 2) {
      const rsrc_t w2r = mkrs(w2 + (size_t)h * 48 * (DM * 2) + j8 * 160, NO_MLP ? 0 : 47 * (DM * 2) + 160);
      u32x2 w[12], bb[3];
      unsigned u[12];
#pragma unroll
      for (int j = 0; j < 12; ++j) {
        wait_until<8>(tb + MLP_T0 + j * MLP_DT);
        LOAD80(w[j], u[j], w2r, kq * (12 * DM * 2) + nn * 8, kq * (12 * DM * 2) + 128 + nn * 2, j * (DM * 2));
        SCHED();
      }
#pragma unroll
      for (int j = 0; j < 3; ++j) bb[j] = *(const u32x2*)(b1 + h * 48 + kq * 12 + j * 4);
      const unsigned ho = WS_QX + (((32 + (h >> 2)) * 4 + (nn >> 2)) * 2 * DH + (h & 3) * 48 + kq * 12) * 8;
      u32x4 z[12];
#pragma nounroll
      for (int it = 0; it < SPIN_MAX; ++it) {
        unsigned bad = 0;
        if (PLAIN && it == 0) {
#pragma unroll
          for (int i = 0; i < 12; ++i) z[i] = bld<0>(wsr, ho + (i % 6) * 16 + (i / 6) * (DH * 8), 0);
        } else {
#pragma unroll
          for (int i = 0; i < 12; ++i) z[i] = bld<SC1>(wsr, ho + (i % 6) * 16 + (i / 6) * (DH * 8), 0);
        }
#pragma unroll
        for (int i = 0; i < 12; ++i) bad |= bad2(z[i], tlo);
        if (__builtin_amdgcn_ballot_w64(bad != 0) == 0) break;
        __builtin_amdgcn_s_sleep(48);
        asm volatile("" ::: "memory");
      }
      f32x4 D[5];
#pragma unroll
      for (int c = 0; c < 5; ++c) D[c] = f32x4{0.f, 0.f, 0.f, 0.f};
#pragma unroll
      for (int j = 0; j < 3; ++j) {
        u32x2 a;
#pragma unroll
        for (int k = 0; k < 2; ++k) {
          const u32x4 p = z[2 * j + k], q = z[6 + 2 * j + k];
          float h0 = rbf(rbf(__uint_as_float(p[0]) + __uint_as_float(q[0])) + bflo(bb[j][k]));
          float h1 = rbf(rbf(__uint_as_float(p[2]) + __uint_as_float(q[2])) + bfhi(bb[j][k]));
          h0 = h0 > 0.f ? h0 : 0.f; h1 = h1 > 0.f ? h1 : 0.f;
          a[k] = pkbf(h0 * h0, h1 * h1);
        }
        const u32x2* v = w + 4 * j;
        D[0] = mfma16(a, u32x2{lolo(v[0][0], v[1][0]), lolo(v[2][0], v[3][0])}, D[0]);
        D[1] = mfma16(a, u32x2{hihi(v[0][0], v[1][0]), hihi(v[2][0], v[3][0])}, D[1]);
        D[2] = mfma16(a, u32x2{lolo(v[0][1], v[1][1]), lolo(v[2][1], v[3][1])}, D[2]);
        D[3] = mfma16(a, u32x2{hihi(v[0][1], v[1][1]), hihi(v[2][1], v[3][1])}, D[3]);
        D[4] = mfma16(a, u32x2{lolo(u[4 * j], u[4 * j + 1]), lolo(u[4 * j + 2], u[4 * j + 3])}, D[4]);
      }
      float* ym = (float*)(lds + L_YM) + kq * 80;
      *(f32x4*)(ym + 4 * nn) = f32x4{D[0][0], D[1][0], D[2][0], D[3][0]};
      ym[64 + nn] = D[4][0];
      lds_post(CT + CT_YM, lane);
    } else if (bid < 160 && (!(DBG & 15) || bid >= 120)) {
      const int rb = bid / 40, col = (bid % 40) * 16 + (lane & 7) * 2;
      const unsigned xv = *(const unsigned*)(x + rb * DM + col), bv = *(const unsigned*)(b2 + col);
      const float r0 = bflo(xv) + bflo(bv), r1 = bfhi(xv) + bfhi(bv);
      lds_wait<1>(CT + CT_PY, rb + 1);
      if (rb < 3) lazy(LAZY); else __builtin_amdgcn_s_sleep(P_W0);
      u32x4 z[4];
#pragma nounroll
      for (int it = 0; it < SPIN_MAX; ++it) {
#pragma unroll
        for (int i = 0; i < 4; ++i) z[i] = bld<SC1>(wsr, WS_Y + bid * (NH * 128) + lane * 16, i * 1024);
        if (__builtin_amdgcn_ballot_w64((bad2(z[0], tlo) | bad2(z[1], tlo) | bad2(z[2], tlo) | bad2(z[3], tlo)) != 0) == 0) break;
        __builtin_amdgcn_s_sleep(P_W1);
        asm volatile("" ::: "memory");
      }
      STAMP(6);
      float s0 = (__uint_as_float(z[0][0]) + __uint_as_float(z[1][0])) + (__uint_as_float(z[2][0]) + __uint_as_float(z[3][0]));
      float s1 = (__uint_as_float(z[0][2]) + __uint_as_float(z[1][2])) + (__uint_as_float(z[2][2]) + __uint_as_float(z[3][2]));
      s0 += dpp<ROR8>(s0); s1 += dpp<ROR8>(s1);
      s0 = rs16(s0, s0); s1 = rs16(s1, s1);
      s0 = rs32(s0, s0); s1 = rs32(s1, s1);

      __builtin_amdgcn_raw_buffer_store_b32(pkbf(r0 + s0, r1 + s1), mkrs(out, NB * DM * 2), lane < 8 ? (rb * DM + col) * 2 : OOB, 0, OUTAUX);
      if (bid == ((DBG & 15) ? 120 : 0)) pub2(wsr, lane == 0 ? WS_EP : OOB, u32x2{tlo, thi});
    }
  } else {
    const int sw = wave - 4, g = lane >> 3, c = lane & 7;
    const size_t base = ((size_t)h * SK + (size_t)j8 * (SK / 8)) * (DH * 2);
    constexpr size_t BSTR = (size_t)NH * SK * DH * 2;
    const unsigned loff = g * (DH * 2) + c * 16;
    int tc = sw + (j8 * 32 + h) * ROTMUL;
    const unsigned po = (((bid >> 3) & 15) == 0 && sw == 0 && lane < 4) ? WS_PC + 4096 + ((bid & 7) * 2 + (bid >> 7)) * 256 + lane * 16 : OOB;
    const unsigned ro = lane < 16 ? WS_PC + lane * 256 : OOB;
    int nt = sw * (TAU16 / 8), dmin = -100000, vo = T0OFF * 16, sg = vo, dv = 0, tcu = TAUC16 / 2, n = 2;
    u32x4 R[16][3];
#define ISSUE(j, r, CM) do { if (WIN) asm volatile("s_waitcnt vmcnt(%0)" :: "n"(WIN) : "memory"); vo += dv; const int d_ = max(vo + nt, dmin); const unsigned o_ = (unsigned)(tc & 127) * TILE_B; \
      const int l_ = wait_until<1>(tb + (unsigned)(d_ >> 4)), f_ = loads_in_flight(); SCHED(); \
      CM; \
      _Pragma("unroll") for (int i_ = 0; i_ < 3; ++i_) R[j][i_] = bld<SAUX>(r, loff + i_ * 128, o_); SCHED(); \
      sg = (sg * 3 + (d_ + l_ * 16 - nt + f_ * (TAU16 / 6))) >> 2; nt += TAU16 / 2; dmin = d_ + l_ * 16 + tcu; tc += 4; SCHED(); } while (0)
#define KTILE(j, J, pk) do { \
      float s0_ = 0.f, s1_ = 0.f, s2_ = 0.f; \
      _Pragma("unroll") for (int k_ = 0; k_ < 4; ++k_) { \
        s0_ = dot2(R[j][0][k_], q[0][k_], s0_); s1_ = dot2(R[j][1][k_], q[1][k_], s1_); s2_ = dot2(R[j][2][k_], q[2][k_], s2_); } \
      float s_ = (s0_ + s1_) + s2_; \
      s_ += dpp<XOR1>(s_); s_ += dpp<XOR2>(s_); s_ += dpp<XOR7>(s_); \
      pk = c == (J) ? s_ : pk; \
      asm volatile("" : "+v"(pk) :: "memory"); SCHED(); } while (0)
#define VTILE(j) do { \
      const float p_ = bcast8<j>(S0); \
      _Pragma("unroll") for (int i_ = 0; i_ < 3; ++i_) _Pragma("unroll") for (int k_ = 0; k_ < 4; ++k_) \
        acc[i_ * 4 + k_] = __builtin_elementwise_fma(f32x2{bflo(R[j][i_][k_]), bfhi(R[j][i_][k_])}, f32x2{p_, p_}, acc[i_ * 4 + k_]); \
      asm volatile("" : "+v"(acc[0]), "+v"(acc[1]), "+v"(acc[2]), "+v"(acc[3]), "+v"(acc[4]), "+v"(acc[5]), \
                        "+v"(acc[6]), "+v"(acc[7]), "+v"(acc[8]), "+v"(acc[9]), "+v"(acc[10]), "+v"(acc[11]) :: "memory"); SCHED(); } while (0)
#define TRC() (void)0
#define CONTROL(j) do { \
      if ((j) == C_AT && C_G == 0) TRC(); \
      if ((j) == C_AT && C_G) { \
        const int d_ = (int)(e - tb * 16u) - vo; \
        int y_ = (d_ > -32000 && d_ < 32000) ? d_ : (n > 2 ? C_MISS : 0); \
        y_ = (y_ + dppi<XOR1>(y_)) >> 1; \
        y_ = max(y_, dppi<XOR2>(y_)); y_ = max(y_, dppi<XOR7>(y_)); y_ = max(y_, dppi<ROR8>(y_)); \
        const int x_ = max(0, min(__builtin_amdgcn_readfirstlane(y_) - C_SLACK, C_CAP)); \
        dv = (x_ * C_G + max(0, x_ - C_K) * C_G2) >> 11; TRC(); SCHED(); } } while (0)
#define CTLMEM(j) do { if (C_G == 0) break; \
      if ((j) == 0) e = __builtin_amdgcn_raw_buffer_load_b32(wsr, ro, n * 4096, 0); \
      if ((j) == 2) { const unsigned v_ = tb * 16u + (unsigned)sg; __builtin_amdgcn_raw_buffer_store_b128(u32x4{v_, v_, v_, v_}, wsr, po, n * 4096, SC1); } } while (0)
    {
      const rsrc_t krs = mkrs(kc + base, PART_B);
#pragma unroll
      for (int j = 0; j < 16; ++j) { if (j < PRE) vo -= T0OFF * 16; ISSUE((j + 8) & 15, krs, (void)0); if (j < PRE) vo += T0OFF * 16; }
    }
    STAMP(1);
    BARRIER();
    int enc = 0;
    if (ENC == 1) enc = (unsigned)__builtin_amdgcn_s_memrealtime() - tb;
    u32x4 q[3];
#pragma unroll
    for (int i = 0; i < 3; ++i) q[i] = *(const u32x4*)(lds + L_QB + 128 * i + 16 * c);
    const unsigned g0 = __builtin_amdgcn_readfirstlane(CT[CT_TAG]), g1 = __builtin_amdgcn_readfirstlane(CT[CT_TAG + 1]);
    STAMP(2);
    float S0 = 0.f, S1 = 0.f, S2 = 0.f, S3 = 0.f;
    {
      const rsrc_t krs = mkrs(kc + base, PART_B);
#pragma unroll
      for (int j = 0; j < 8; ++j) { KTILE(j, j, S3); ISSUE(j, krs, (void)0); KTILE(8 + j, j, S2); }
    }
    int it = 2;
#pragma nounroll
    for (int b = 0; b < NB; ++b) {
#pragma nounroll
      for (; it < 4; ++it, ++n) {
        const rsrc_t r = mkrs((it < 3 ? kc : vc) + base + b * BSTR, PART_B);
        unsigned e;
        S0 = S1; S1 = S2; S2 = S3;
#pragma unroll
        for (int j = 0; j < 8; ++j) { KTILE(j, j, S3); CONTROL(j); ISSUE(j, r, CTLMEM(j)); }
      }
      {
        S0 = rbf(rbf(S0) * SCALE); S1 = rbf(rbf(S1) * SCALE); S2 = rbf(rbf(S2) * SCALE); S3 = rbf(rbf(S3) * SCALE);
        float m = fmaxf(fmaxf(S0, S1), fmaxf(S2, S3));
        m = fmaxf(m, dpp<XOR1>(m)); m = fmaxf(m, dpp<XOR2>(m)); m = fmaxf(m, dpp<XOR7>(m));
        m = max_x16_32(fmaxf(m, dpp<ROR8>(m)));
        S0 = __builtin_amdgcn_exp2f((S0 - m) * LOG2E); S1 = __builtin_amdgcn_exp2f((S1 - m) * LOG2E);
        S2 = __builtin_amdgcn_exp2f((S2 - m) * LOG2E); S3 = __builtin_amdgcn_exp2f((S3 - m) * LOG2E);
        const float l = wave_sum((S0 + S1) + (S2 + S3));
        const unsigned u = __float_as_uint(lane & 1 ? l : m);
        pub4(wsr, lane < 2 ? WS_MX + (b * NH + h) * 1024 + (j8 * 4 + sw) * 32 + lane * 16 : OOB,
             u32x4{g0 | (u & 0xffffu), g1, g0 | (u >> 16), g1});
        lds_post(CT + CT_ML, lane);
      }
      f32x2 acc[12];
#pragma unroll
      for (int k = 0; k < 12; ++k) acc[k] = f32x2{0.f, 0.f};
#pragma nounroll
      for (it = 0; it < 4; ++it, ++n) {
        const rsrc_t r = mkrs(it < 3 ? vc + base + b * BSTR : kc + base + (b < 3 ? b + 1 : b) * BSTR, n == 31 ? 0 : PART_B);
        if (n == 31) { nt -= 1 << 24; dmin = nt; tcu = -(1 << 24); }
        unsigned e;
        VTILE(0); CONTROL(0); ISSUE(0, r, CTLMEM(0)); VTILE(1); CONTROL(1); ISSUE(1, r, CTLMEM(1));
        VTILE(2); CONTROL(2); ISSUE(2, r, CTLMEM(2)); VTILE(3); CONTROL(3); ISSUE(3, r, CTLMEM(3));
        VTILE(4); CONTROL(4); ISSUE(4, r, CTLMEM(4)); VTILE(5); CONTROL(5); ISSUE(5, r, CTLMEM(5));
        VTILE(6); CONTROL(6); ISSUE(6, r, CTLMEM(6)); VTILE(7); CONTROL(7); ISSUE(7, r, CTLMEM(7));
        S0 = S1; S1 = S2; S2 = S3;
      }
      if (b == 3) STAMP(3);
      if (ENC == 2 && b == 0) enc = vo >> 4;
      if (ENC == 3 && b == 3) enc = vo >> 4;
      if (ENC == 4 && b == 3) enc = (unsigned)__builtin_amdgcn_s_memrealtime() - tb - 11000;
      if (ENC == 5 && b == 1) enc = vo >> 4;
      float* const OA = (float*)(lds + L_OA + b * 3072) + sw * 192 + 8 * c + g;
#pragma unroll
      for (int i = 0; i < 3; ++i) {
        float u[4], v[2];
#pragma unroll
        for (int k = 0; k < 4; ++k) u[k] = rs32(acc[i * 4 + (k >> 1)][k & 1], acc[i * 4 + 2 + (k >> 1)][k & 1]);
#pragma unroll
        for (int k = 0; k < 2; ++k) v[k] = rs16(u[k], u[k + 2]);
        OA[64 * i] = rs8(v[0], v[1], lane & 8);
      }
      lds_post(CT + CT_OA + b, lane);
      if (b < 3) {
        lds_wait<1>(CT + CT_Q, 3);
#pragma unroll
        for (int i = 0; i < 3; ++i) q[i] = *(const u32x4*)(lds + L_QB + (b + 1) * 384 + 128 * i + 16 * c);
      }
      it = 0;
    }
    if (ENC) wait_until<1>(tb + 15000 + 10 * enc);
  }
  if (wave == 0) {
    const u32x4 v = bld<SC1>(mkrs(ws + (32 << 20), 1024), lane * 16, 0);
    int cn = 0;
    for (int i = 0; i < 4; ++i) { int d = (int)(v[i] - tp); if (d > -5000 && d < 5000 && lane >= PLO && lane < PHI) cn += v[i] & 7; }
    for (int o = 1; o < 64; o <<= 1) cn += __shfl_xor(cn, o);
    wait_until<1>(tp + 15000 + __builtin_amdgcn_readfirstlane(cn) * 10);
  }
  STAMP(4);
}

extern "C" void kernel_launch(void* const* d_in, const int* in_sizes, int n_in, void* d_out, int out_size, void* d_ws,
                              size_t ws_size, hipStream_t stream) {
  (void)in_sizes; (void)n_in; (void)out_size; (void)ws_size;
  fused_block<<<NWG, NTHR, 0, stream>>>((const unsigned short*)d_in[0], (const unsigned char*)d_in[1],
                                        (const unsigned char*)d_in[2], (const unsigned char*)d_in[3],
                                        (const unsigned char*)d_in[4], (const unsigned char*)d_in[6],
                                        (const unsigned short*)d_in[7], (const unsigned char*)d_in[8],
                                        (const unsigned short*)d_in[9], (unsigned char*)d_ws, (unsigned short*)d_out);
}
```

```cpp
#define TAU16 1365
#include <hip/hip_runtime.h>
#include <hip/hip_bf16.h>
#include <stdint.h>

typedef unsigned u32x4 __attribute__((ext_vector_type(4)));
typedef unsigned u32x2 __attribute__((ext_vector_type(2)));
typedef float f32x4 __attribute__((ext_vector_type(4)));
typedef float f32x2 __attribute__((ext_vector_type(2)));
typedef __bf16 bf16x2 __attribute__((ext_vector_type(2)));
typedef __bf16 bf16x8 __attribute__((ext_vector_type(8)));
typedef short s16x4 __attribute__((ext_vector_type(4)));
typedef __amdgpu_buffer_rsrc_t rsrc_t;

constexpr int NB = 4, DM = 640, NH = 32, SK = 8192, DH = 192, FF = 1536;
constexpr int NWG = 256, NTHR = 512;
constexpr int TILE_B = 8 * DH * 2;
constexpr int PART_B = 128 * TILE_B;
#define ROTMUL 21
#define TAUC16 600
#define T0OFF 200
#define FORGIVE 100000
#define C_SLACK 1100
#define C_G 80
#define C_K 2400
#define C_G2 200
#define C_MISS 4800
#define C_AT 6
#define C_CAP 6400
#define WO_T0 1200
#define WO_DT 90
#define MLP_T0 1300
#define MLP_DT 100
#define LAZY 400
#define P_W0 5
#define P_W1 2
#define R_W0 2
#define R_W1 P_W1
#define PIPE 0
#define P_X 188
#define R_X 274
#define KSCH 0
#define EARLY 1
#define WQ_AUX 0
#define WO_AUX 0
#define Q_W0 24
#define Q_W1 24
#define Q_T0 480
#define WIN 0
#define T0MLP T0OFF
#define NO_MLP 0
#define NO_WO 0
#define WQ_SLP 2
#define PRE 1
#define PLAIN 1
#define DBG 0
#define Q_T1 900
#define SAUX 2
#define OUTAUX 16
#define ENC 0
#define PLO 0
#define PHI 64
#define PTHR 60
#define NEAR 0
#define NEARM 3
#define TS 0
#define FAUX (NEAR ? 1 : 0)
#define OAUX FAUX
#define OSAUX SC1
#define NEARQ(it) ((NEAR & 2) ? ((it) & NEARM) != NEARM : (it) == 0)
#define NEARO(it, b) ((NEAR & 1) ? ((it) & NEARM) != NEARM : ((it) == 0 && (b) < 3))
constexpr int NT = 2, SC1 = 16;
constexpr float LOG2E = 1.4426950408889634f;
constexpr float SCALE = 0.07216878364870322f;
constexpr unsigned OOB = 0x40000000u;
constexpr int SPIN_MAX = 1 << 18;

constexpr unsigned WS_EP = 0;
constexpr unsigned WS_PC = 4096;
constexpr unsigned WS_QX = WS_PC + 33 * 4096;
constexpr unsigned WS_OX = WS_QX + 40 * NB * 2 * DH * 8;
constexpr unsigned WS_Y = WS_OX + NB * NH * 7168;
constexpr unsigned WS_END = WS_Y + NB * 40 * NH * 128;

constexpr int L_PP = 0;
constexpr int L_QB = L_PP + 4096;
constexpr int L_OA = L_QB + 1536;
constexpr int L_YM = L_OA + 4 * 3072;
constexpr int L_ML = L_YM + 1280;
constexpr int L_CT = L_ML + 128;
constexpr int L_END = L_CT + 64;
enum { CT_QP, CT_Q, CT_ML, CT_PO, CT_YM, CT_PY, CT_ST, CT_OA = 8, CT_TL = 12 };

__device__ __forceinline__ rsrc_t mkrs(const void* p, unsigned bytes) {
  return __builtin_amdgcn_make_buffer_rsrc((void*)p, 0, bytes, 0x00020000);
}
template <int AUX> __device__ __forceinline__ u32x4 bld(rsrc_t r, unsigned voff, unsigned soff) {
  return __builtin_amdgcn_raw_buffer_load_b128(r, voff, soff, AUX);
}
__device__ __forceinline__ void pub4(rsrc_t r, unsigned voff, u32x4 v) { __builtin_amdgcn_raw_buffer_store_b128(v, r, voff, 0, SC1); }
__device__ __forceinline__ void pub2(rsrc_t r, unsigned voff, u32x2 v) { __builtin_amdgcn_raw_buffer_store_b64(v, r, voff, 0, SC1); }
__device__ __forceinline__ float dot2(unsigned a, unsigned b, float c) {
  return __builtin_amdgcn_fdot2_f32_bf16(__builtin_bit_cast(bf16x2, a), __builtin_bit_cast(bf16x2, b), c, false);
}
__device__ __forceinline__ f32x4 mfma(u32x4 a, u32x4 b, f32x4 c) {
  return __builtin_amdgcn_mfma_f32_16x16x32_bf16(__builtin_bit_cast(bf16x8, a), __builtin_bit_cast(bf16x8, b), c, 0, 0, 0);
}
__device__ __forceinline__ f32x4 mfma16(u32x2 a, u32x2 b, f32x4 c) {
  return __builtin_amdgcn_mfma_f32_16x16x16bf16_1k(__builtin_bit_cast(s16x4, a), __builtin_bit_cast(s16x4, b), c, 0, 0, 0);
}
__device__ __forceinline__ float bflo(unsigned v) { return __builtin_bit_cast(float, v << 16); }
__device__ __forceinline__ float bfhi(unsigned v) { return __builtin_bit_cast(float, v & 0xffff0000u); }
__device__ __forceinline__ float rbf(float x) { return (float)(__bf16)x; }
__device__ __forceinline__ unsigned pkbf(float lo, float hi) {
  bf16x2 v = {(__bf16)lo, (__bf16)hi};
  return __builtin_bit_cast(unsigned, v);
}
__device__ __forceinline__ unsigned ln2(unsigned v, float mu, float rs) {
  const unsigned d = pkbf(bflo(v) - mu, bfhi(v) - mu);
  return pkbf(bflo(d) * rs, bfhi(d) * rs);
}
__device__ __forceinline__ unsigned lolo(unsigned a, unsigned b) { return __builtin_amdgcn_perm(b, a, 0x05040100u); }
__device__ __forceinline__ unsigned hihi(unsigned a, unsigned b) { return __builtin_amdgcn_perm(b, a, 0x07060302u); }
template <int CTRL> __device__ __forceinline__ float dpp(float x) {
  return __builtin_bit_cast(float, __builtin_amdgcn_mov_dpp(__builtin_bit_cast(int, x), CTRL, 0xf, 0xf, true));
}
constexpr int XOR1 = 0xB1, XOR2 = 0x4E, XOR7 = 0x141, ROR4 = 0x124, ROR8 = 0x128;
__device__ __forceinline__ float rs32(float a, float b) {
  auto s = __builtin_amdgcn_permlane32_swap(__float_as_uint(a), __float_as_uint(b), false, false);
  return __uint_as_float(s[0]) + __uint_as_float(s[1]);
}
__device__ __forceinline__ float rs16(float a, float b) {
  auto s = __builtin_amdgcn_permlane16_swap(__float_as_uint(a), __float_as_uint(b), false, false);
  return __uint_as_float(s[0]) + __uint_as_float(s[1]);
}
__device__ __forceinline__ float rs8(float a, float b, bool up) {
  const float t1 = a + dpp<ROR8>(a), t2 = b + dpp<ROR8>(b);
  return up ? t2 : t1;
}
__device__ __forceinline__ float max_x16_32(float x) {
  auto s = __builtin_amdgcn_permlane16_swap(__float_as_uint(x), __float_as_uint(x), false, false);
  x = fmaxf(__uint_as_float(s[0]), __uint_as_float(s[1]));
  auto t = __builtin_amdgcn_permlane32_swap(__float_as_uint(x), __float_as_uint(x), false, false);
  return fmaxf(__uint_as_float(t[0]), __uint_as_float(t[1]));
}
__device__ __forceinline__ float sum_q16(float x) {
  x += dpp<ROR4>(x); x += dpp<ROR8>(x);
  x = rs16(x, x);
  return rs32(x, x);
}
__device__ __forceinline__ float wave_sum(float x) {
  x += dpp<XOR1>(x); x += dpp<XOR2>(x);
  return sum_q16(x);
}
template <int SLP> __device__ __forceinline__ int wait_until(unsigned due) {
  int late;
  asm volatile("L_pace_%=:\n\t"
               "s_memrealtime vcc\n\t"
               "s_waitcnt lgkmcnt(0)\n\t"
               "s_sub_i32 vcc_lo, vcc_lo, %1\n\t"
               "s_cmp_lt_i32 vcc_lo, 0\n\t"
               "s_cbranch_scc0 L_done_%=\n\t"
               "s_sleep %2\n\t"
               "s_branch L_pace_%=\n"
               "L_done_%=:\n\t"
               "s_mov_b32 %0, vcc_lo"
               : "=s"(late) : "s"(due), "n"(SLP) : "scc", "vcc", "memory");
  return late;
}
__device__ __forceinline__ int loads_in_flight() {
  unsigned ib;
  asm volatile("s_getreg_b32 %0, hwreg(HW_REG_IB_STS)" : "=s"(ib));
  return (ib & 15) | ((ib >> 30) << 4);
}
template <int CTRL> __device__ __forceinline__ int dppi(int x) { return __builtin_amdgcn_mov_dpp(x, CTRL, 0xf, 0xf, true); }
template <int J> __device__ __forceinline__ float bcast8(float x) {
  const int v = __builtin_bit_cast(int, x);
  const int lo = __builtin_amdgcn_update_dpp(0, v, 0x150 + J, 0xf, 0x3, false);
  return __builtin_bit_cast(float, __builtin_amdgcn_update_dpp(lo, v, 0x158 + J, 0xf, 0xc, false));
}
#define SCHED() __builtin_amdgcn_sched_barrier(0)
#define LDS_FENCE() asm volatile("s_waitcnt lgkmcnt(0)" ::: "memory")
#define DRAIN() asm volatile("s_waitcnt vmcnt(0)" ::: "memory")
#define BARRIER() do { SCHED(); LDS_FENCE(); __builtin_amdgcn_s_barrier(); asm volatile("" ::: "memory"); SCHED(); } while (0)
#define STAMP(i) do {} while (0)

__device__ __forceinline__ unsigned lds_arrive(unsigned* cnt, int lane) {
  LDS_FENCE();
  unsigned old = 0;
  if (lane == 0) old = __hip_atomic_fetch_add(cnt, 1u, __ATOMIC_RELAXED, __HIP_MEMORY_SCOPE_WORKGROUP);
  old = __builtin_amdgcn_readfirstlane(old);
  asm volatile("" ::: "memory");
  return old;
}
__device__ __forceinline__ void lds_post(unsigned* cnt, int lane) {
  LDS_FENCE();
  if (lane == 0) __hip_atomic_fetch_add(cnt, 1u, __ATOMIC_RELAXED, __HIP_MEMORY_SCOPE_WORKGROUP);
  asm volatile("" ::: "memory");
}
template <int SLP> __device__ __forceinline__ void lds_wait(unsigned* cnt, unsigned n) {
  while (__builtin_amdgcn_readfirstlane(__hip_atomic_load(cnt, __ATOMIC_RELAXED, __HIP_MEMORY_SCOPE_WORKGROUP)) < n)
    __builtin_amdgcn_s_sleep(SLP);
  asm volatile("" ::: "memory");
}
__device__ __forceinline__ void lazy(int ticks) { wait_until<8>((unsigned)__builtin_amdgcn_s_memrealtime() + ticks); }
__device__ __forceinline__ unsigned bad2(u32x4 v, unsigned tag) { return (v[1] ^ tag) | (v[3] ^ tag); }
#define LOAD80(w, u, r, vo1, vo2, so) do { w = __builtin_amdgcn_raw_buffer_load_b64(r, vo1, so, WO_AUX); \
    u = __builtin_amdgcn_raw_buffer_load_b16(r, vo2, so, WO_AUX); } while (0)

__device__ __forceinline__ void fetch_q(rsrc_t wsr, unsigned char* lds, int h, int n, unsigned tag, int lane) {
  const unsigned qo = WS_QX + (h * 4 + n) * (2 * DH * 8) + lane * 16, qo2 = qo + 1024 + (lane >> 5) * 1024;
  u32x4 qa, qb, qc;
#pragma nounroll
  for (int it = 0; it < SPIN_MAX; ++it) {
    if (NEARQ(it)) { qa = bld<FAUX>(wsr, qo, 0); qb = bld<FAUX>(wsr, qo, DH * 8); qc = bld<FAUX>(wsr, qo2, 0); }
    else { qa = bld<SC1>(wsr, qo, 0); qb = bld<SC1>(wsr, qo, DH * 8); qc = bld<SC1>(wsr, qo2, 0); }
    const unsigned bad = bad2(qa, tag) | bad2(qb, tag) | bad2(qc, tag);
    if (__builtin_amdgcn_ballot_w64(bad != 0) == 0) break;
    __builtin_amdgcn_s_sleep(Q_W1);
    asm volatile("" ::: "memory");
  }
  unsigned* QB = (unsigned*)(lds + L_QB + n * 384);
  QB[lane] = pkbf(__uint_as_float(qa[0]) + __uint_as_float(qb[0]), __uint_as_float(qa[2]) + __uint_as_float(qb[2]));
  const float c0 = __uint_as_float(qc[0]), c1 = __uint_as_float(qc[2]);
  const unsigned t = pkbf(rs32(c0, c0), rs32(c1, c1));
  if (lane < 32) QB[64 + lane] = t;
}
__device__ __forceinline__ float sum_row16(float x) {
  x += dpp<XOR1>(x); x += dpp<XOR2>(x); x += dpp<XOR7>(x);
  return x + dpp<ROR8>(x);
}

__global__ void __launch_bounds__(NTHR, 2)
fused_block(const unsigned short* __restrict__ x, const unsigned char* __restrict__ kc, const unsigned char* __restrict__ vc,
            const unsigned char* __restrict__ wq, const unsigned char* __restrict__ wo, const unsigned char* __restrict__ w1,
            const unsigned short* __restrict__ b1, const unsigned char* __restrict__ w2, const unsigned short* __restrict__ b2,
            unsigned char* ws, unsigned short* out) {
  __shared__ __attribute__((aligned(16))) unsigned char lds[L_END];
  const unsigned long long tb64 = __builtin_amdgcn_s_memrealtime();
  const unsigned tb = (unsigned)tb64;
  const int tid = threadIdx.x, lane = tid & 63, wave = __builtin_amdgcn_readfirstlane(tid >> 6);
  const int bid = blockIdx.x;
  const int h = (bid & 7) * 4 + ((bid >> 3) & 3), j8 = bid >> 5;
  unsigned* const CT = (unsigned*)(lds + L_CT);
  const rsrc_t wsr = mkrs(ws, WS_END);
  if (tid < 16) CT[tid] = 0;
  BARRIER();

  if (wave < 4) {
    const int hw = wave;
    const int kq = lane >> 4, nn = lane & 15;
    const bool mlp = j8 >= 6;
    const int half = mlp ? j8 & 1 : j8 / 3, grp = mlp ? 32 + (h >> 2) : h, sub = mlp ? h & 3 : j8 % 3, qr = half * 320 + hw * 80 + kq * 20;
    const unsigned rstr = mlp ? FF * 2 : NH * DH * 2;
    const rsrc_t xr = mkrs(x, NB * DM * 2);
    u32x4 xs[5];
    u32x2 xa[5], wq2[20];
#pragma unroll
    for (int i = 0; i < 5; ++i) xs[i] = bld<0>(xr, kq * (DM * 2) + nn * 16, i * 256);
    const rsrc_t wqr = mkrs((mlp ? w1 : wq) + (grp & 31) * 384 + sub * 128, sub < 3 ? (DM - 1) * rstr + 128 : 0);
#pragma unroll
    for (int i = 0; i < 5; ++i) xa[i] = __builtin_amdgcn_raw_buffer_load_b64(xr, (nn >> 2) * (DM * 2) + qr * 2, i * 8, 0);
    const u32x2 ep = __builtin_amdgcn_raw_buffer_load_b64(wsr, WS_EP, 0, SC1);
    const unsigned wqo = qr * rstr + nn * 8;
#pragma unroll
    for (int j = 0; j < 20; ++j) { wq2[j] = __builtin_amdgcn_raw_buffer_load_b64(wqr, wqo, j * rstr, WQ_AUX); if (WQ_SLP) { SCHED(); __builtin_amdgcn_s_sleep(WQ_SLP); SCHED(); } }
    SCHED();
    float sm = 0.f;
#pragma unroll
    for (int i = 0; i < 5; ++i)
#pragma unroll
      for (int k = 0; k < 4; ++k) sm = dot2(xs[i][k], 0x3f803f80u, sm);
    const float mean = sum_row16(sm) * (1.f / DM), mub = rbf(mean);
    f32x2 sq = {0.f, 0.f};
#pragma unroll
    for (int i = 0; i < 5; ++i)
#pragma unroll
      for (int k = 0; k < 4; ++k) {
        const f32x2 d = f32x2{bflo(xs[i][k]), bfhi(xs[i][k])} - f32x2{mean, mean};
        sq = __builtin_elementwise_fma(d, d, sq);
      }
    const float sd = rbf(__fsqrt_rn(rbf(rbf(sum_row16(sq[0] + sq[1]) * (1.f / DM)) + 1e-5f)));
    const float rsd = 1.f / sd;
    u32x2 A[5];
    {
      const float ma = __shfl(mub, (lane & 12) * 4), ra = __shfl(rsd, (lane & 12) * 4);
#pragma unroll
      for (int i = 0; i < 5; ++i) A[i] = u32x2{ln2(xa[i][0], ma, ra), ln2(xa[i][1], ma, ra)};
    }
    SCHED();
    {
      f32x4 D[4];
#pragma unroll
      for (int c = 0; c < 4; ++c) D[c] = f32x4{0.f, 0.f, 0.f, 0.f};
#pragma unroll
      for (int j = 0; j < 5; ++j) {
        const u32x2* w = wq2 + 4 * j;
        D[0] = mfma16(A[j], u32x2{lolo(w[0][0], w[1][0]), lolo(w[2][0], w[3][0])}, D[0]);
        D[1] = mfma16(A[j], u32x2{hihi(w[0][0], w[1][0]), hihi(w[2][0], w[3][0])}, D[1]);
        D[2] = mfma16(A[j], u32x2{lolo(w[0][1], w[1][1]), lolo(w[2][1], w[3][1])}, D[2]);
        D[3] = mfma16(A[j], u32x2{hihi(w[0][1], w[1][1]), hihi(w[2][1], w[3][1])}, D[3]);
      }
      *(f32x4*)(lds + L_PP + hw * 1024 + lane * 16) = f32x4{D[0][0], D[1][0], D[2][0], D[3][0]};
    }
    const unsigned long long T = (((unsigned long long)__builtin_amdgcn_readfirstlane(ep[1]) << 32) |
                                  (unsigned)__builtin_amdgcn_readfirstlane(ep[0])) + 1ull;
    const unsigned tlo = (unsigned)T, thi = (unsigned)(T >> 32);
    if (lds_arrive(CT + CT_QP, lane) == 3) {
      STAMP(7);
      const f32x4* pp = (const f32x4*)(lds + L_PP) + lane;
      const u32x4 s = __builtin_bit_cast(u32x4, (pp[0] + pp[64]) + (pp[128] + pp[192]));
      const unsigned qo = sub < 3 ? WS_QX + (((grp * 4 + kq) * 2 + half) * DH + sub * 64 + nn * 4) * 8 : OOB;
      pub4(wsr, qo, u32x4{s[0], tlo, s[1], tlo});
      pub4(wsr, qo + 16, u32x4{s[2], tlo, s[3], tlo});
      __builtin_amdgcn_s_sleep(Q_W0);
      wait_until<2>(tb + Q_T0);
      fetch_q(wsr, lds, h, 0, tlo, lane);
      STAMP(1);
    }
    BARRIER();
    if (hw > 0) {
      wait_until<8>(tb + Q_T1);
      fetch_q(wsr, lds, h, hw, tlo, lane);
      lds_post(CT + CT_Q, lane);
    }
    if (hw == 0) {
#pragma nounroll
      for (int b = 0; b < NB; ++b) {
        lds_wait<16>(CT + CT_ML, 4 * b + 4);
        const f32x4 ma = *(const f32x4*)(lds + L_ML + b * 32), mb = *(const f32x4*)(lds + L_ML + b * 32 + 16);
        float Mw = fmaxf(fmaxf(ma[0], ma[2]), fmaxf(mb[0], mb[2]));
        float w0 = __builtin_amdgcn_exp2f((ma[0] - Mw) * LOG2E), w1_ = __builtin_amdgcn_exp2f((ma[2] - Mw) * LOG2E);
        float w2_ = __builtin_amdgcn_exp2f((mb[0] - Mw) * LOG2E), w3 = __builtin_amdgcn_exp2f((mb[2] - Mw) * LOG2E);
        float Lw = (w0 * ma[1] + w1_ * ma[3]) + (w2_ * mb[1] + w3 * mb[3]);
        const f32x4* oa = (const f32x4*)(lds + L_OA + b * 3072) + (lane < 48 ? (lane >> 3) * 4 + (lane & 4) * 6 + (lane & 3) : 0);
        pub4(wsr, lane < 8 ? WS_OX + (b * NH + h) * 7168 + 6144 + j8 * 128 + lane * 16 : OOB, u32x4{__float_as_uint(Mw), tlo, __float_as_uint(Lw), tlo});
        if (EARLY) lds_post(CT + CT_ST, lane);
        unsigned oo = lane < 48 ? WS_OX + (b * NH + h) * 7168 + (lane >> 3) * 1024 + j8 * 128 + (lane & 7) * 16 : OOB;
        asm volatile("" : "+v"(w0), "+v"(w1_), "+v"(w2_), "+v"(w3), "+v"(oo));
        lds_wait<1>(CT + CT_OA + b, 4);
        if (b == 3) STAMP(3);
        const f32x4 r = (oa[0] * w0 + oa[48] * w1_) + (oa[96] * w2_ + oa[144] * w3);
        pub4(wsr, oo, u32x4{pkbf(r[0], r[1]), tlo, pkbf(r[2], r[3]), tlo});
        lds_post(CT + CT_PO, lane);
        if (TS == 3 && b == 3) STAMP(2);
      }
    } else if (hw == 1) {
      const rsrc_t wor = mkrs(wo + (size_t)h * DH * (DM * 2) + j8 * 160, NO_WO ? 0 : (DH - 1) * (DM * 2) + 160);
      const unsigned wo1 = kq * (4 * DM * 2) + nn * 8, wo2 = kq * (4 * DM * 2) + 128 + nn * 2;
      u32x4 F[6][5];
#pragma unroll
      for (int kb = 0; kb < 6; ++kb) {
        u32x2 w[8];
        unsigned u[8];
#pragma unroll
        for (int j = 0; j < 8; ++j) {
          wait_until<8>(tb + WO_T0 + (8 * kb + j) * WO_DT);
          LOAD80(w[j], u[j], wor, wo1, wo2, (32 * kb + 16 * (j >> 2) + (j & 3)) * (DM * 2));
          SCHED();
        }
#pragma unroll
        for (int k = 0; k < 2; ++k) {
          F[kb][2 * k] = u32x4{lolo(w[0][k], w[1][k]), lolo(w[2][k], w[3][k]), lolo(w[4][k], w[5][k]), lolo(w[6][k], w[7][k])};
          F[kb][2 * k + 1] = u32x4{hihi(w[0][k], w[1][k]), hihi(w[2][k], w[3][k]), hihi(w[4][k], w[5][k]), hihi(w[6][k], w[7][k])};
        }
        F[kb][4] = u32x4{lolo(u[0], u[1]), lolo(u[2], u[3]), lolo(u[4], u[5]), lolo(u[6], u[7])};
        asm volatile("" : "+v"(F[kb][0]), "+v"(F[kb][1]), "+v"(F[kb][2]), "+v"(F[kb][3]), "+v"(F[kb][4]) :: "memory");
        SCHED();
      }
      lds_wait<8>(CT + CT_YM, 1);
#pragma nounroll
      for (int b = 0; b < NB; ++b) {
        const float* ym = (const float*)(lds + L_YM) + b * 80;
        const float m0 = ym[4 * nn + kq], m1 = ym[64 + nn];
        const unsigned oo = WS_OX + (b * NH + h) * 7168 + nn * 64 + kq * 16;
        const unsigned so = WS_OX + (b * NH + h) * 7168 + 6144 + ((2 * kq + nn) & 7) * 128 + (lane >> 3) * 16;
        const unsigned yo = WS_Y + ((b * 40 + j8 * 5) * NH + h) * 128;
        float wa, wb;
        auto weights = [&](u32x4 st) {
          const float mj = __uint_as_float(st[0]);
          float Mh = fmaxf(mj, dpp<XOR1>(mj));
          Mh = fmaxf(Mh, dpp<XOR2>(Mh));
          Mh = fmaxf(Mh, dpp<XOR7>(Mh));
          const float ej = __builtin_amdgcn_exp2f((mj - Mh) * LOG2E);
          float Lh = ej * __uint_as_float(st[2]);
          Lh += dpp<XOR1>(Lh); Lh += dpp<XOR2>(Lh); Lh += dpp<XOR7>(Lh);
          const float wj = EARLY ? ej / Lh : ej * __builtin_amdgcn_rcpf(Lh);
          wa = dpp<0x150>(wj); wb = dpp<0x151>(wj);
        };
        if (EARLY) {
          lds_wait<16>(CT + CT_ST, b + 1);
          lazy(LAZY);
          u32x4 st;
#pragma nounroll
          for (int it = 0; it < SPIN_MAX; ++it) {
            if (it == 0) st = bld<0>(wsr, so, 0); else st = bld<SC1>(wsr, so, 0);
            if (__builtin_amdgcn_ballot_w64(bad2(st, tlo) != 0) == 0) break;
            __builtin_amdgcn_s_sleep(48);
            asm volatile("" ::: "memory");
          }
          weights(st);
          asm volatile("" : "+v"(wa), "+v"(wb));
        }
        lds_wait<1>(CT + CT_PO, b + 1);
        if (TS == 1 && b == 3) STAMP(2);
        if (b < 3) lazy(LAZY);
        else { __builtin_amdgcn_s_sleep(P_W0); if (P_X) wait_until<1>(__builtin_amdgcn_readfirstlane(CT[CT_TL]) + P_X); }
        float t[5];
#pragma nounroll
        for (int it = 0; it < SPIN_MAX; ++it) {
          u32x4 z[6], st;
          if (it == 0 && b < 3) {
            if (!EARLY) st = bld<0>(wsr, so, 0);
#pragma unroll
            for (int i = 0; i < 6; ++i) z[i] = bld<0>(wsr, oo, i * 1024);
          } else {
            if (!EARLY) st = bld<SC1>(wsr, so, 0);
#pragma unroll
            for (int i = 0; i < 6; ++i) z[i] = bld<SC1>(wsr, oo, i * 1024);
          }
          SCHED();
          if (!EARLY) weights(st);
          f32x4 D[10];
          unsigned bad = EARLY ? 0 : bad2(st, tlo);
#pragma unroll
          for (int c = 0; c < 10; ++c) D[c] = f32x4{0.f, 0.f, 0.f, 0.f};
#pragma unroll
          for (int kb = 0; kb < 3; ++kb) {
            const u32x4 a = {z[2 * kb][0], z[2 * kb][2], z[2 * kb + 1][0], z[2 * kb + 1][2]};
            bad |= bad2(z[2 * kb], tlo) | bad2(z[2 * kb + 1], tlo);
#pragma unroll
            for (int c = 0; c < 5; ++c) { D[c] = mfma(a, F[kb][c], D[c]); D[5 + c] = mfma(a, F[kb + 3][c], D[5 + c]); }
            if (KSCH) SCHED();
          }
#pragma unroll
          for (int c = 0; c < 5; ++c) t[c] = wa * (D[c][0] + D[5 + c][1]) + wb * (D[c][2] + D[5 + c][3]);
          if (__builtin_amdgcn_ballot_w64(bad != 0) == 0) {
            break;
          }
          __builtin_amdgcn_s_sleep(P_W1);
          asm volatile("" ::: "memory");
        }
        if (b == 3) STAMP(5);
        const float y0 = rs16(rs32(t[0], t[2]), rs32(t[1], t[3])) + m0;
        t[4] = rs32(t[4], t[4]);
        const float y1 = rs16(t[4], t[4]) + m1;
        pub2(wsr, yo + (nn >> 2) * (NH * 128) + ((nn & 3) * 4 + kq) * 8, u32x2{__float_as_uint(y0), tlo});
        pub2(wsr, lane < 16 ? yo + 4 * (NH * 128) + nn * 8 : OOB, u32x2{__float_as_uint(y1), tlo});
        lds_post(CT + CT_PY, lane);
        if (TS == 4 && b == 3) STAMP(2);
      }
    } else if (hw == 2) {
      const rsrc_t w2r = mkrs(w2 + (size_t)h * 48 * (DM * 2) + j8 * 160, NO_MLP ? 0 : 47 * (DM * 2) + 160);
      u32x2 w[12], bb[3];
      unsigned u[12];
#pragma unroll
      for (int j = 0; j < 12; ++j) {
        wait_until<8>(tb + MLP_T0 + j * MLP_DT);
        LOAD80(w[j], u[j], w2r, kq * (12 * DM * 2) + nn * 8, kq * (12 * DM * 2) + 128 + nn * 2, j * (DM * 2));
        SCHED();
      }
#pragma unroll
      for (int j = 0; j < 3; ++j) bb[j] = *(const u32x2*)(b1 + h * 48 + kq * 12 + j * 4);
      const unsigned ho = WS_QX + (((32 + (h >> 2)) * 4 + (nn >> 2)) * 2 * DH + (h & 3) * 48 + kq * 12) * 8;
      u32x4 z[12];
#pragma nounroll
      for (int it = 0; it < SPIN_MAX; ++it) {
        unsigned bad = 0;
        if (NEARQ(it)) {
#pragma unroll
          for (int i = 0; i < 12; ++i) z[i] = bld<FAUX>(wsr, ho + (i % 6) * 16 + (i / 6) * (DH * 8), 0);
        } else {
#pragma unroll
          for (int i = 0; i < 12; ++i) z[i] = bld<SC1>(wsr, ho + (i % 6) * 16 + (i / 6) * (DH * 8), 0);
        }
#pragma unroll
        for (int i = 0; i < 12; ++i) bad |= bad2(z[i], tlo);
        if (__builtin_amdgcn_ballot_w64(bad != 0) == 0) break;
        __builtin_amdgcn_s_sleep(48);
        asm volatile("" ::: "memory");
      }
      f32x4 D[5];
#pragma unroll
      for (int c = 0; c < 5; ++c) D[c] = f32x4{0.f, 0.f, 0.f, 0.f};
#pragma unroll
      for (int j = 0; j < 3; ++j) {
        u32x2 a;
#pragma unroll
        for (int k = 0; k < 2; ++k) {
          const u32x4 p = z[2 * j + k], q = z[6 + 2 * j + k];
          float h0 = rbf(rbf(__uint_as_float(p[0]) + __uint_as_float(q[0])) + bflo(bb[j][k]));
          float h1 = rbf(rbf(__uint_as_float(p[2]) + __uint_as_float(q[2])) + bfhi(bb[j][k]));
          h0 = h0 > 0.f ? h0 : 0.f; h1 = h1 > 0.f ? h1 : 0.f;
          a[k] = pkbf(h0 * h0, h1 * h1);
        }
        const u32x2* v = w + 4 * j;
        D[0] = mfma16(a, u32x2{lolo(v[0][0], v[1][0]), lolo(v[2][0], v[3][0])}, D[0]);
        D[1] = mfma16(a, u32x2{hihi(v[0][0], v[1][0]), hihi(v[2][0], v[3][0])}, D[1]);
        D[2] = mfma16(a, u32x2{lolo(v[0][1], v[1][1]), lolo(v[2][1], v[3][1])}, D[2]);
        D[3] = mfma16(a, u32x2{hihi(v[0][1], v[1][1]), hihi(v[2][1], v[3][1])}, D[3]);
        D[4] = mfma16(a, u32x2{lolo(u[4 * j], u[4 * j + 1]), lolo(u[4 * j + 2], u[4 * j + 3])}, D[4]);
      }
      float* ym = (float*)(lds + L_YM) + kq * 80;
      *(f32x4*)(ym + 4 * nn) = f32x4{D[0][0], D[1][0], D[2][0], D[3][0]};
      ym[64 + nn] = D[4][0];
      lds_post(CT + CT_YM, lane);
    } else if (bid < 160) {
      const int rb = bid / 40, col = (bid % 40) * 16 + (lane & 7) * 2;
      const unsigned xv = *(const unsigned*)(x + rb * DM + col), bv = *(const unsigned*)(b2 + col);
      const float r0 = bflo(xv) + bflo(bv), r1 = bfhi(xv) + bfhi(bv);
      lds_wait<1>(CT + CT_PY, rb + 1);
      if (rb < 3) lazy(LAZY); else { __builtin_amdgcn_s_sleep(R_W0); if (R_X) wait_until<1>(__builtin_amdgcn_readfirstlane(CT[CT_TL]) + R_X); }
      if (TS == 5 && rb == 3) STAMP(2);
      u32x4 z[4];
#pragma nounroll
      for (int it = 0; it < SPIN_MAX; ++it) {
#pragma unroll
        for (int i = 0; i < 4; ++i) z[i] = bld<SC1>(wsr, WS_Y + bid * (NH * 128) + lane * 16, i * 1024);
        if (__builtin_amdgcn_ballot_w64((bad2(z[0], tlo) | bad2(z[1], tlo) | bad2(z[2], tlo) | bad2(z[3], tlo)) != 0) == 0) {
          break;
        }
        __builtin_amdgcn_s_sleep(R_W1);
        asm volatile("" ::: "memory");
      }
      STAMP(6);
      float s0 = (__uint_as_float(z[0][0]) + __uint_as_float(z[1][0])) + (__uint_as_float(z[2][0]) + __uint_as_float(z[3][0]));
      float s1 = (__uint_as_float(z[0][2]) + __uint_as_float(z[1][2])) + (__uint_as_float(z[2][2]) + __uint_as_float(z[3][2]));
      s0 += dpp<ROR8>(s0); s1 += dpp<ROR8>(s1);
      s0 = rs16(s0, s0); s1 = rs16(s1, s1);
      s0 = rs32(s0, s0); s1 = rs32(s1, s1);

      __builtin_amdgcn_raw_buffer_store_b32(pkbf(r0 + s0, r1 + s1), mkrs(out, NB * DM * 2), lane < 8 ? (rb * DM + col) * 2 : OOB, 0, OUTAUX);
      if (bid == 0) pub2(wsr, lane == 0 ? WS_EP : OOB, u32x2{tlo, thi});
    }
  } else {
    const int sw = wave - 4, g = lane >> 3, c = lane & 7;
    const size_t base = ((size_t)h * SK + (size_t)j8 * (SK / 8)) * (DH * 2);
    constexpr size_t BSTR = (size_t)NH * SK * DH * 2;
    const unsigned loff = g * (DH * 2) + c * 16;
    int tc = sw + (j8 * 32 + h) * ROTMUL;
    const unsigned po = (((bid >> 3) & 15) == 0 && sw == 0 && lane < 4) ? WS_PC + 4096 + ((bid & 7) * 2 + (bid >> 7)) * 256 + lane * 16 : OOB;
    const unsigned ro = lane < 16 ? WS_PC + lane * 256 : OOB;
    int nt = sw * (TAU16 / 8), dmin = -100000, vo = T0OFF * 16, sg = vo, dv = 0, tcu = TAUC16 / 2, n = 2;
    u32x4 R[16][3];
#define ISSUE(j, r, CM) do { if (WIN) asm volatile("s_waitcnt vmcnt(%0)" :: "n"(WIN) : "memory"); vo += dv; const int d_ = max(vo + nt, dmin); const unsigned o_ = (unsigned)(tc & 127) * TILE_B; \
      const int l_ = wait_until<1>(tb + (unsigned)(d_ >> 4)), f_ = loads_in_flight(); SCHED(); \
      CM; \
      _Pragma("unroll") for (int i_ = 0; i_ < 3; ++i_) R[j][i_] = bld<SAUX>(r, loff + i_ * 128, o_); SCHED(); \
      sg = (sg * 3 + (d_ + l_ * 16 - nt + f_ * (TAU16 / 6))) >> 2; nt += TAU16 / 2; dmin = d_ + l_ * 16 + tcu; tc += 4; SCHED(); } while (0)
#define KTILE(j, J, pk) do { \
      float s0_ = 0.f, s1_ = 0.f, s2_ = 0.f; \
      _Pragma("unroll") for (int k_ = 0; k_ < 4; ++k_) { \
        s0_ = dot2(R[j][0][k_], q[0][k_], s0_); s1_ = dot2(R[j][1][k_], q[1][k_], s1_); s2_ = dot2(R[j][2][k_], q[2][k_], s2_); } \
      float s_ = (s0_ + s1_) + s2_; \
      s_ += dpp<XOR1>(s_); s_ += dpp<XOR2>(s_); s_ += dpp<XOR7>(s_); \
      pk = c == (J) ? s_ : pk; \
      asm volatile("" : "+v"(pk) :: "memory"); SCHED(); } while (0)
#define VTILE(j) do { \
      const float p_ = bcast8<j>(S0); \
      _Pragma("unroll") for (int i_ = 0; i_ < 3; ++i_) _Pragma("unroll") for (int k_ = 0; k_ < 4; ++k_) \
        acc[i_ * 4 + k_] = __builtin_elementwise_fma(f32x2{bflo(R[j][i_][k_]), bfhi(R[j][i_][k_])}, f32x2{p_, p_}, acc[i_ * 4 + k_]); \
      asm volatile("" : "+v"(acc[0]), "+v"(acc[1]), "+v"(acc[2]), "+v"(acc[3]), "+v"(acc[4]), "+v"(acc[5]), \
                        "+v"(acc[6]), "+v"(acc[7]), "+v"(acc[8]), "+v"(acc[9]), "+v"(acc[10]), "+v"(acc[11]) :: "memory"); SCHED(); } while (0)
#define TRC() (void)0
#define CONTROL(j) do { \
      if ((j) == C_AT && C_G == 0) TRC(); \
      if ((j) == C_AT && C_G) { \
        const int d_ = (int)(e - tb * 16u) - vo; \
        int y_ = (d_ > -32000 && d_ < 32000) ? d_ : (n > 2 ? C_MISS : 0); \
        y_ = (y_ + dppi<XOR1>(y_)) >> 1; \
        y_ = max(y_, dppi<XOR2>(y_)); y_ = max(y_, dppi<XOR7>(y_)); y_ = max(y_, dppi<ROR8>(y_)); \
        const int x_ = max(0, min(__builtin_amdgcn_readfirstlane(y_) - C_SLACK, C_CAP)); \
        dv = (x_ * C_G + max(0, x_ - C_K) * C_G2) >> 11; TRC(); SCHED(); } } while (0)
#define CTLMEM(j) do { if (C_G == 0) break; \
      if ((j) == 0) e = __builtin_amdgcn_raw_buffer_load_b32(wsr, ro, n * 4096, 0); \
      if ((j) == 2) { const unsigned v_ = tb * 16u + (unsigned)sg; __builtin_amdgcn_raw_buffer_store_b128(u32x4{v_, v_, v_, v_}, wsr, po, n * 4096, SC1); } } while (0)
    {
      const rsrc_t krs = mkrs(kc + base, PART_B);
#pragma unroll
      for (int j = 0; j < 16; ++j) { if (j < PRE) vo -= T0OFF * 16; ISSUE((j + 8) & 15, krs, (void)0); if (j < PRE) vo += T0OFF * 16; }
    }
    STAMP(1);
    BARRIER();
    int enc = 0;
    if (ENC == 1) enc = (unsigned)__builtin_amdgcn_s_memrealtime() - tb;
    u32x4 q[3];
#pragma unroll
    for (int i = 0; i < 3; ++i) q[i] = *(const u32x4*)(lds + L_QB + 128 * i + 16 * c);
    STAMP(2);
    float S0 = 0.f, S1 = 0.f, S2 = 0.f, S3 = 0.f;
    {
      const rsrc_t krs = mkrs(kc + base, PART_B);
#pragma unroll
      for (int j = 0; j < 8; ++j) { KTILE(j, j, S3); ISSUE(j, krs, (void)0); KTILE(8 + j, j, S2); }
    }
    int it = 2;
#pragma nounroll
    for (int b = 0; b < NB; ++b) {
#pragma nounroll
      for (; it < 4; ++it, ++n) {
        const rsrc_t r = mkrs((it < 3 ? kc : vc) + base + b * BSTR, PART_B);
        unsigned e;
        S0 = S1; S1 = S2; S2 = S3;
#pragma unroll
        for (int j = 0; j < 8; ++j) { KTILE(j, j, S3); CONTROL(j); ISSUE(j, r, CTLMEM(j)); }
      }
      {
        S0 = rbf(rbf(S0) * SCALE); S1 = rbf(rbf(S1) * SCALE); S2 = rbf(rbf(S2) * SCALE); S3 = rbf(rbf(S3) * SCALE);
        float m = fmaxf(fmaxf(S0, S1), fmaxf(S2, S3));
        m = fmaxf(m, dpp<XOR1>(m)); m = fmaxf(m, dpp<XOR2>(m)); m = fmaxf(m, dpp<XOR7>(m));
        m = max_x16_32(fmaxf(m, dpp<ROR8>(m)));
        S0 = __builtin_amdgcn_exp2f((S0 - m) * LOG2E); S1 = __builtin_amdgcn_exp2f((S1 - m) * LOG2E);
        S2 = __builtin_amdgcn_exp2f((S2 - m) * LOG2E); S3 = __builtin_amdgcn_exp2f((S3 - m) * LOG2E);
        const float l = wave_sum((S0 + S1) + (S2 + S3));
        if (lane == 0) *(f32x2*)(lds + L_ML + b * 32 + sw * 8) = f32x2{m, l};
        lds_post(CT + CT_ML, lane);
      }
      f32x2 acc[12];
#pragma unroll
      for (int k = 0; k < 12; ++k) acc[k] = f32x2{0.f, 0.f};
#pragma nounroll
      for (it = 0; it < 4; ++it, ++n) {
        const rsrc_t r = mkrs(it < 3 ? vc + base + b * BSTR : kc + base + (b < 3 ? b + 1 : b) * BSTR, n == 31 ? 0 : PART_B);
        if (n == 31) { if (sw == 3) CT[CT_TL] = tb + ((vo + nt) >> 4); nt -= 1 << 24; dmin = nt; tcu = -(1 << 24); }
        unsigned e;
        VTILE(0); CONTROL(0); ISSUE(0, r, CTLMEM(0)); VTILE(1); CONTROL(1); ISSUE(1, r, CTLMEM(1));
        VTILE(2); CONTROL(2); ISSUE(2, r, CTLMEM(2)); VTILE(3); CONTROL(3); ISSUE(3, r, CTLMEM(3));
        VTILE(4); CONTROL(4); ISSUE(4, r, CTLMEM(4)); VTILE(5); CONTROL(5); ISSUE(5, r, CTLMEM(5));
        VTILE(6); CONTROL(6); ISSUE(6, r, CTLMEM(6)); VTILE(7); CONTROL(7); ISSUE(7, r, CTLMEM(7));
        S0 = S1; S1 = S2; S2 = S3;
      }
      if (b == 3) STAMP(3);
      if (ENC == 2 && b == 0) enc = vo >> 4;
      if (ENC == 3 && b == 3) enc = vo >> 4;
      if (ENC == 4 && b == 3) enc = (unsigned)__builtin_amdgcn_s_memrealtime() - tb - 11000;
      if (ENC == 5 && b == 1) enc = vo >> 4;
      float* const OA = (float*)(lds + L_OA + b * 3072) + sw * 192 + 8 * c + g;
#pragma unroll
      for (int i = 0; i < 3; ++i) {
        float u[4], v[2];
#pragma unroll
        for (int k = 0; k < 4; ++k) u[k] = rs32(acc[i * 4 + (k >> 1)][k & 1], acc[i * 4 + 2 + (k >> 1)][k & 1]);
#pragma unroll
        for (int k = 0; k < 2; ++k) v[k] = rs16(u[k], u[k + 2]);
        OA[64 * i] = rs8(v[0], v[1], lane & 8);
      }
      lds_post(CT + CT_OA + b, lane);
      if (b < 3) {
        lds_wait<1>(CT + CT_Q, 3);
#pragma unroll
        for (int i = 0; i < 3; ++i) q[i] = *(const u32x4*)(lds + L_QB + (b + 1) * 384 + 128 * i + 16 * c);
      }
      it = 0;
    }
    if (ENC) wait_until<1>(tb + 15000 + 10 * enc);
  }
  STAMP(4);
}

extern "C" void kernel_launch(void* const* d_in, const int* in_sizes, int n_in, void* d_out, int out_size, void* d_ws,
                              size_t ws_size, hipStream_t stream) {
  (void)in_sizes; (void)n_in; (void)out_size; (void)ws_size;
  fused_block<<<NWG, NTHR, 0, stream>>>((const unsigned short*)d_in[0], (const unsigned char*)d_in[1],
                                        (const unsigned char*)d_in[2], (const unsigned char*)d_in[3],
                                        (const unsigned char*)d_in[4], (const unsigned char*)d_in[6],
                                        (const unsigned short*)d_in[7], (const unsigned char*)d_in[8],
                                        (const unsigned short*)d_in[9], (unsigned char*)d_ws, (unsigned short*)d_out);
}
```

```cpp
#include <hip/hip_runtime.h>
#include <hip/hip_bf16.h>
#include <stdint.h>

typedef unsigned u32x4 __attribute__((ext_vector_type(4)));
typedef unsigned u32x2 __attribute__((ext_vector_type(2)));
typedef float f32x4 __attribute__((ext_vector_type(4)));
typedef float f32x2 __attribute__((ext_vector_type(2)));
typedef __bf16 bf16x2 __attribute__((ext_vector_type(2)));
typedef __bf16 bf16x8 __attribute__((ext_vector_type(8)));
typedef short s16x4 __attribute__((ext_vector_type(4)));
typedef __amdgpu_buffer_rsrc_t rsrc_t;

constexpr int NB = 4, DM = 640, NH = 32, SK = 8192, DH = 192, FF = 1536;
constexpr int NWG = 256, NTHR = 512;
constexpr int TILE_B = 8 * DH * 2;
constexpr int GW = 8;
constexpr int TMASK = 128 * GW - 1, TSTEP = 4 * GW;
constexpr int PART_B = (TMASK + 1) * TILE_B;
constexpr int RA = 27;
constexpr int TAU16 = 1310;
constexpr int TAUC16 = 600;
constexpr int T0OFF = 200;
constexpr int PRE = 2;
constexpr int PF3 = 4;
constexpr int C_SLACK = 650, C_G = 60, C_K = 1200, C_G2 = 100, C_MISS = 4800, C_CAP = 7000, C_NEG = 1000;
constexpr int C_AT = 6, C_WR = 2;
constexpr int WO_T0 = 1200, WO_DT = 25, MLP_T0 = 1300, MLP_DT = 100;
constexpr int LAZY = 400;
constexpr int Q_W0 = 24, Q_W1 = 24;
constexpr int Q_T0 = 480, Q_T1 = 900;
constexpr int P_W0 = 5, P_W1 = 2, R_W0 = 2, R_W1 = 2;
constexpr int P_X = 196, R_X = 282, Y_L = 150, R_L = 250;
constexpr int NT = 2, SC1 = 16;
constexpr float LOG2E = 1.4426950408889634f;
constexpr float SCALE = 0.07216878364870322f;
constexpr unsigned OOB = 0x40000000u;
constexpr int SPIN_MAX = 1 << 18;

constexpr unsigned WS_EP = 0;
constexpr unsigned WS_PC = 4096;
constexpr unsigned WS_QX = WS_PC + 33 * 4096;
constexpr unsigned WS_OX = WS_QX + 40 * NB * 2 * DH * 8;
constexpr unsigned WS_Y = WS_OX + NB * NH * 7168;
constexpr unsigned WS_END = WS_Y + NB * 40 * NH * 128;

constexpr int L_PP = 0;
constexpr int L_QB = L_PP + 4096;
constexpr int L_OA = L_QB + 1536;
constexpr int L_YM = L_OA + 4 * 3072;
constexpr int L_ML = L_YM + 1280;
constexpr int L_CT = L_ML + 128;
constexpr int L_END = L_CT + 64;
enum { CT_QP, CT_Q, CT_ML, CT_PO, CT_YM, CT_PY, CT_ST, CT_OA = 8, CT_TL = 12, CT_TG = 13 };

__device__ __forceinline__ rsrc_t mkrs(const void* p, unsigned bytes) {
  return __builtin_amdgcn_make_buffer_rsrc((void*)p, 0, bytes, 0x00020000);
}
template <int AUX> __device__ __forceinline__ u32x4 bld(rsrc_t r, unsigned voff, unsigned soff) {
  return __builtin_amdgcn_raw_buffer_load_b128(r, voff, soff, AUX);
}
__device__ __forceinline__ void pub4(rsrc_t r, unsigned voff, u32x4 v) { __builtin_amdgcn_raw_buffer_store_b128(v, r, voff, 0, SC1); }
__device__ __forceinline__ void pub2(rsrc_t r, unsigned voff, u32x2 v) { __builtin_amdgcn_raw_buffer_store_b64(v, r, voff, 0, SC1); }
__device__ __forceinline__ float dot2(unsigned a, unsigned b, float c) {
  return __builtin_amdgcn_fdot2_f32_bf16(__builtin_bit_cast(bf16x2, a), __builtin_bit_cast(bf16x2, b), c, false);
}
__device__ __forceinline__ f32x4 mfma(u32x4 a, u32x4 b, f32x4 c) {
  return __builtin_amdgcn_mfma_f32_16x16x32_bf16(__builtin_bit_cast(bf16x8, a), __builtin_bit_cast(bf16x8, b), c, 0, 0, 0);
}
__device__ __forceinline__ f32x4 mfma16(u32x2 a, u32x2 b, f32x4 c) {
  return __builtin_amdgcn_mfma_f32_16x16x16bf16_1k(__builtin_bit_cast(s16x4, a), __builtin_bit_cast(s16x4, b), c, 0, 0, 0);
}
__device__ __forceinline__ float bflo(unsigned v) { return __builtin_bit_cast(float, v << 16); }
__device__ __forceinline__ float bfhi(unsigned v) { return __builtin_bit_cast(float, v & 0xffff0000u); }
__device__ __forceinline__ float rbf(float x) { return (float)(__bf16)x; }
__device__ __forceinline__ unsigned pkbf(float lo, float hi) {
  bf16x2 v = {(__bf16)lo, (__bf16)hi};
  return __builtin_bit_cast(unsigned, v);
}
__device__ __forceinline__ unsigned ln2(unsigned v, float mu, float rs) {
  const unsigned d = pkbf(bflo(v) - mu, bfhi(v) - mu);
  return pkbf(bflo(d) * rs, bfhi(d) * rs);
}
__device__ __forceinline__ unsigned lolo(unsigned a, unsigned b) { return __builtin_amdgcn_perm(b, a, 0x05040100u); }
__device__ __forceinline__ unsigned hihi(unsigned a, unsigned b) { return __builtin_amdgcn_perm(b, a, 0x07060302u); }
template <int CTRL> __device__ __forceinline__ float dpp(float x) {
  return __builtin_bit_cast(float, __builtin_amdgcn_mov_dpp(__builtin_bit_cast(int, x), CTRL, 0xf, 0xf, true));
}
constexpr int XOR1 = 0xB1, XOR2 = 0x4E, XOR7 = 0x141, ROR4 = 0x124, ROR8 = 0x128;
__device__ __forceinline__ float rs32(float a, float b) {
  auto s = __builtin_amdgcn_permlane32_swap(__float_as_uint(a), __float_as_uint(b), false, false);
  return __uint_as_float(s[0]) + __uint_as_float(s[1]);
}
__device__ __forceinline__ float rs16(float a, float b) {
  auto s = __builtin_amdgcn_permlane16_swap(__float_as_uint(a), __float_as_uint(b), false, false);
  return __uint_as_float(s[0]) + __uint_as_float(s[1]);
}
__device__ __forceinline__ float rs8(float a, float b, bool up) {
  const float t1 = a + dpp<ROR8>(a), t2 = b + dpp<ROR8>(b);
  return up ? t2 : t1;
}
__device__ __forceinline__ float max_x16_32(float x) {
  auto s = __builtin_amdgcn_permlane16_swap(__float_as_uint(x), __float_as_uint(x), false, false);
  x = fmaxf(__uint_as_float(s[0]), __uint_as_float(s[1]));
  auto t = __builtin_amdgcn_permlane32_swap(__float_as_uint(x), __float_as_uint(x), false, false);
  return fmaxf(__uint_as_float(t[0]), __uint_as_float(t[1]));
}
__device__ __forceinline__ float sum_q16(float x) {
  x += dpp<ROR4>(x); x += dpp<ROR8>(x);
  x = rs16(x, x);
  return rs32(x, x);
}
__device__ __forceinline__ float wave_sum(float x) {
  x += dpp<XOR1>(x); x += dpp<XOR2>(x);
  return sum_q16(x);
}
template <int SLP> __device__ __forceinline__ int wait_until(unsigned due) {
  int late;
  asm volatile("L_pace_%=:\n\t"
               "s_memrealtime vcc\n\t"
               "s_waitcnt lgkmcnt(0)\n\t"
               "s_sub_i32 vcc_lo, vcc_lo, %1\n\t"
               "s_cmp_lt_i32 vcc_lo, 0\n\t"
               "s_cbranch_scc0 L_done_%=\n\t"
               "s_sleep %2\n\t"
               "s_branch L_pace_%=\n"
               "L_done_%=:\n\t"
               "s_mov_b32 %0, vcc_lo"
               : "=s"(late) : "s"(due), "n"(SLP) : "scc", "vcc", "memory");
  return late;
}
__device__ __forceinline__ int loads_in_flight() {
  unsigned ib;
  asm volatile("s_getreg_b32 %0, hwreg(HW_REG_IB_STS)" : "=s"(ib));
  return (ib & 15) | ((ib >> 30) << 4);
}
template <int CTRL> __device__ __forceinline__ int dppi(int x) { return __builtin_amdgcn_mov_dpp(x, CTRL, 0xf, 0xf, true); }
template <int J> __device__ __forceinline__ float bcast8(float x) {
  const int v = __builtin_bit_cast(int, x);
  const int lo = __builtin_amdgcn_update_dpp(0, v, 0x150 + J, 0xf, 0x3, false);
  return __builtin_bit_cast(float, __builtin_amdgcn_update_dpp(lo, v, 0x158 + J, 0xf, 0xc, false));
}
#define SCHED() __builtin_amdgcn_sched_barrier(0)
#define LDS_FENCE() asm volatile("s_waitcnt lgkmcnt(0)" ::: "memory")
#define BARRIER() do { SCHED(); LDS_FENCE(); __builtin_amdgcn_s_barrier(); asm volatile("" ::: "memory"); SCHED(); } while (0)

__device__ __forceinline__ unsigned lds_arrive(unsigned* cnt, int lane) {
  LDS_FENCE();
  unsigned old = 0;
  if (lane == 0) old = __hip_atomic_fetch_add(cnt, 1u, __ATOMIC_RELAXED, __HIP_MEMORY_SCOPE_WORKGROUP);
  old = __builtin_amdgcn_readfirstlane(old);
  asm volatile("" ::: "memory");
  return old;
}
__device__ __forceinline__ void lds_post(unsigned* cnt, int lane) {
  LDS_FENCE();
  if (lane == 0) __hip_atomic_fetch_add(cnt, 1u, __ATOMIC_RELAXED, __HIP_MEMORY_SCOPE_WORKGROUP);
  asm volatile("" ::: "memory");
}
template <int SLP> __device__ __forceinline__ void lds_wait(unsigned* cnt, unsigned n) {
  while ((unsigned)__builtin_amdgcn_readfirstlane(__hip_atomic_load(cnt, __ATOMIC_RELAXED, __HIP_MEMORY_SCOPE_WORKGROUP)) < n)
    __builtin_amdgcn_s_sleep(SLP);
  asm volatile("" ::: "memory");
}
__device__ __forceinline__ void lazy(int ticks) { wait_until<8>((unsigned)__builtin_amdgcn_s_memrealtime() + ticks); }
__device__ __forceinline__ unsigned bad2(u32x4 v, unsigned tag) { return (v[1] ^ tag) | (v[3] ^ tag); }
#define LOAD80(w, u, r, vo1, vo2, so) do { w = __builtin_amdgcn_raw_buffer_load_b64(r, vo1, so, 0); \
    u = __builtin_amdgcn_raw_buffer_load_b16(r, vo2, so, 0); } while (0)

__device__ __forceinline__ void fetch_q(rsrc_t wsr, unsigned char* lds, int h, int n, unsigned tag, int lane) {
  const unsigned qo = WS_QX + (h * 4 + n) * (2 * DH * 8) + lane * 16, qo2 = qo + 1024 + (lane >> 5) * 1024;
  u32x4 qa, qb, qc;
#pragma nounroll
  for (int it = 0; it < SPIN_MAX; ++it) {
    if (it == 0) { qa = bld<0>(wsr, qo, 0); qb = bld<0>(wsr, qo, DH * 8); qc = bld<0>(wsr, qo2, 0); }
    else { qa = bld<SC1>(wsr, qo, 0); qb = bld<SC1>(wsr, qo, DH * 8); qc = bld<SC1>(wsr, qo2, 0); }
    const unsigned bad = bad2(qa, tag) | bad2(qb, tag) | bad2(qc, tag);
    if (__builtin_amdgcn_ballot_w64(bad != 0) == 0) break;
    __builtin_amdgcn_s_sleep(Q_W1);
    asm volatile("" ::: "memory");
  }
  unsigned* QB = (unsigned*)(lds + L_QB + n * 384);
  QB[lane] = pkbf(__uint_as_float(qa[0]) + __uint_as_float(qb[0]), __uint_as_float(qa[2]) + __uint_as_float(qb[2]));
  const float c0 = __uint_as_float(qc[0]), c1 = __uint_as_float(qc[2]);
  const unsigned t = pkbf(rs32(c0, c0), rs32(c1, c1));
  if (lane < 32) QB[64 + lane] = t;
}
__device__ __forceinline__ float sum_row16(float x) {
  x += dpp<XOR1>(x); x += dpp<XOR2>(x); x += dpp<XOR7>(x);
  return x + dpp<ROR8>(x);
}

__global__ void __launch_bounds__(NTHR, 2)
fused_block(const unsigned short* __restrict__ x, const unsigned char* __restrict__ kc, const unsigned char* __restrict__ vc,
            const unsigned char* __restrict__ wq, const unsigned char* __restrict__ wo, const unsigned char* __restrict__ w1,
            const unsigned short* __restrict__ b1, const unsigned char* __restrict__ w2, const unsigned short* __restrict__ b2,
            unsigned char* ws, unsigned short* out) {
  __shared__ __attribute__((aligned(16))) unsigned char lds[L_END];
  const unsigned tb = (unsigned)__builtin_amdgcn_s_memrealtime();
  const int tid = threadIdx.x, lane = tid & 63, wave = __builtin_amdgcn_readfirstlane(tid >> 6);
  const int bid = blockIdx.x;
  const int h = (bid & 7) * 4 + ((bid >> 3) & 3), j8 = bid >> 5;
  unsigned* const CT = (unsigned*)(lds + L_CT);
  const rsrc_t wsr = mkrs(ws, WS_END);
  if (tid < 16) CT[tid] = 0;
  BARRIER();

  if (wave < 4) {
    const int hw = wave;
    const int kq = lane >> 4, nn = lane & 15;
    const bool mlp = j8 >= 6;
    const int half = mlp ? j8 & 1 : j8 / 3, grp = mlp ? 32 + (h >> 2) : h, sub = mlp ? h & 3 : j8 % 3, qr = half * 320 + hw * 80 + kq * 20;
    const unsigned rstr = mlp ? FF * 2 : NH * DH * 2;
    const rsrc_t xr = mkrs(x, NB * DM * 2);
    u32x4 xs[5];
    u32x2 xa[5], wq2[20];
#pragma unroll
    for (int i = 0; i < 5; ++i) xs[i] = bld<0>(xr, kq * (DM * 2) + nn * 16, i * 256);
    const rsrc_t wqr = mkrs((mlp ? w1 : wq) + (grp & 31) * 384 + sub * 128, sub < 3 ? (DM - 1) * rstr + 128 : 0);
#pragma unroll
    for (int i = 0; i < 5; ++i) xa[i] = __builtin_amdgcn_raw_buffer_load_b64(xr, (nn >> 2) * (DM * 2) + qr * 2, i * 8, 0);
    const u32x2 ep = __builtin_amdgcn_raw_buffer_load_b64(wsr, WS_EP, 0, SC1);
    const unsigned wqo = qr * rstr + nn * 8;
#pragma unroll
    for (int j = 0; j < 20; ++j) { wq2[j] = __builtin_amdgcn_raw_buffer_load_b64(wqr, wqo, j * rstr, 0); SCHED(); __builtin_amdgcn_s_sleep(2); SCHED(); }
    SCHED();
    float sm = 0.f;
#pragma unroll
    for (int i = 0; i < 5; ++i)
#pragma unroll
      for (int k = 0; k < 4; ++k) sm = dot2(xs[i][k], 0x3f803f80u, sm);
    const float mean = sum_row16(sm) * (1.f / DM), mub = rbf(mean);
    f32x2 sq = {0.f, 0.f};
#pragma unroll
    for (int i = 0; i < 5; ++i)
#pragma unroll
      for (int k = 0; k < 4; ++k) {
        const f32x2 d = f32x2{bflo(xs[i][k]), bfhi(xs[i][k])} - f32x2{mean, mean};
        sq = __builtin_elementwise_fma(d, d, sq);
      }
    const float sd = rbf(__fsqrt_rn(rbf(rbf(sum_row16(sq[0] + sq[1]) * (1.f / DM)) + 1e-5f)));
    const float rsd = 1.f / sd;
    u32x2 A[5];
    {
      const float ma = __shfl(mub, (lane & 12) * 4), ra = __shfl(rsd, (lane & 12) * 4);
#pragma unroll
      for (int i = 0; i < 5; ++i) A[i] = u32x2{ln2(xa[i][0], ma, ra), ln2(xa[i][1], ma, ra)};
    }
    SCHED();
    {
      f32x4 D[4];
#pragma unroll
      for (int c = 0; c < 4; ++c) D[c] = f32x4{0.f, 0.f, 0.f, 0.f};
#pragma unroll
      for (int j = 0; j < 5; ++j) {
        const u32x2* w = wq2 + 4 * j;
        D[0] = mfma16(A[j], u32x2{lolo(w[0][0], w[1][0]), lolo(w[2][0], w[3][0])}, D[0]);
        D[1] = mfma16(A[j], u32x2{hihi(w[0][0], w[1][0]), hihi(w[2][0], w[3][0])}, D[1]);
        D[2] = mfma16(A[j], u32x2{lolo(w[0][1], w[1][1]), lolo(w[2][1], w[3][1])}, D[2]);
        D[3] = mfma16(A[j], u32x2{hihi(w[0][1], w[1][1]), hihi(w[2][1], w[3][1])}, D[3]);
      }
      *(f32x4*)(lds + L_PP + hw * 1024 + lane * 16) = f32x4{D[0][0], D[1][0], D[2][0], D[3][0]};
    }
    const unsigned long long T = (((unsigned long long)__builtin_amdgcn_readfirstlane(ep[1]) << 32) |
                                  (unsigned)__builtin_amdgcn_readfirstlane(ep[0])) + 1ull;
    const unsigned tlo = (unsigned)T, thi = (unsigned)(T >> 32);
    if (lds_arrive(CT + CT_QP, lane) == 3) {
      const f32x2* pp = (const f32x2*)(lds + L_PP) + kq * 32 + nn;
      const u32x2 s0 = __builtin_bit_cast(u32x2, (pp[0] + pp[128]) + (pp[256] + pp[384]));
      const u32x2 s1 = __builtin_bit_cast(u32x2, (pp[16] + pp[144]) + (pp[272] + pp[400]));
      const unsigned qo = sub < 3 ? WS_QX + (((grp * 4 + kq) * 2 + half) * DH + sub * 64 + nn * 2) * 8 : OOB;
      pub4(wsr, qo, u32x4{s0[0], tlo, s0[1], tlo});
      pub4(wsr, qo + 256, u32x4{s1[0], tlo, s1[1], tlo});
      __builtin_amdgcn_s_sleep(Q_W0);
      wait_until<2>(tb + Q_T0);
      fetch_q(wsr, lds, h, 0, tlo, lane);
    }
    BARRIER();
    if (hw > 0) {
      wait_until<8>(tb + Q_T1);
      fetch_q(wsr, lds, h, hw, tlo, lane);
      lds_post(CT + CT_Q, lane);
    }
    if (hw == 0) {
#pragma nounroll
      for (int b = 0; b < NB; ++b) {
        lds_wait<16>(CT + CT_ML, 4 * b + 4);
        const f32x4 ma = *(const f32x4*)(lds + L_ML + b * 32), mb = *(const f32x4*)(lds + L_ML + b * 32 + 16);
        float Mw = fmaxf(fmaxf(ma[0], ma[2]), fmaxf(mb[0], mb[2]));
        float w0 = __builtin_amdgcn_exp2f((ma[0] - Mw) * LOG2E), w1_ = __builtin_amdgcn_exp2f((ma[2] - Mw) * LOG2E);
        float w2_ = __builtin_amdgcn_exp2f((mb[0] - Mw) * LOG2E), w3 = __builtin_amdgcn_exp2f((mb[2] - Mw) * LOG2E);
        float Lw = (w0 * ma[1] + w1_ * ma[3]) + (w2_ * mb[1] + w3 * mb[3]);
        const f32x4* oa = (const f32x4*)(lds + L_OA + b * 3072) + (lane < 48 ? (lane >> 3) * 4 + (lane & 4) * 6 + (lane & 3) : 0);
        pub4(wsr, lane < 8 ? WS_OX + (b * NH + h) * 7168 + 6144 + j8 * 128 + lane * 16 : OOB, u32x4{__float_as_uint(Mw), tlo, __float_as_uint(Lw), tlo});
        lds_post(CT + CT_ST, lane);
        unsigned oo = lane < 48 ? WS_OX + (b * NH + h) * 7168 + (lane >> 3) * 1024 + j8 * 128 + (lane & 7) * 16 : OOB;
        asm volatile("" : "+v"(w0), "+v"(w1_), "+v"(w2_), "+v"(w3), "+v"(oo));
        lds_wait<1>(CT + CT_OA + b, 4);
        const f32x4 r = (oa[0] * w0 + oa[48] * w1_) + (oa[96] * w2_ + oa[144] * w3);
        pub4(wsr, oo, u32x4{pkbf(r[0], r[1]), tlo, pkbf(r[2], r[3]), tlo});
        lds_post(CT + CT_PO, lane);
      }
    } else if (hw == 1) {
      const rsrc_t wor = mkrs(wo + (size_t)h * DH * (DM * 2) + j8 * 160, (DH - 1) * (DM * 2) + 160);
      const unsigned wo1 = kq * (4 * DM * 2) + nn * 8, wo2 = kq * (4 * DM * 2) + 128 + nn * 2;
      u32x4 F[6][5];
#pragma unroll
      for (int kb = 0; kb < 6; ++kb) {
        u32x2 w[8];
        unsigned u[8];
#pragma unroll
        for (int j = 0; j < 8; ++j) {
          wait_until<8>(tb + WO_T0 + (8 * kb + j) * WO_DT);
          LOAD80(w[j], u[j], wor, wo1, wo2, (32 * kb + 16 * (j >> 2) + (j & 3)) * (DM * 2));
          SCHED();
        }
#pragma unroll
        for (int k = 0; k < 2; ++k) {
          F[kb][2 * k] = u32x4{lolo(w[0][k], w[1][k]), lolo(w[2][k], w[3][k]), lolo(w[4][k], w[5][k]), lolo(w[6][k], w[7][k])};
          F[kb][2 * k + 1] = u32x4{hihi(w[0][k], w[1][k]), hihi(w[2][k], w[3][k]), hihi(w[4][k], w[5][k]), hihi(w[6][k], w[7][k])};
        }
        F[kb][4] = u32x4{lolo(u[0], u[1]), lolo(u[2], u[3]), lolo(u[4], u[5]), lolo(u[6], u[7])};
        asm volatile("" : "+v"(F[kb][0]), "+v"(F[kb][1]), "+v"(F[kb][2]), "+v"(F[kb][3]), "+v"(F[kb][4]) :: "memory");
        SCHED();
      }
      lds_wait<8>(CT + CT_YM, 1);
#pragma nounroll
      for (int b = 0; b < NB; ++b) {
        const float* ym = (const float*)(lds + L_YM) + b * 80;
        const float m0 = ym[lane], m1 = ym[64 + nn];
        const unsigned oo = WS_OX + (b * NH + h) * 7168 + nn * 64 + kq * 16;
        const unsigned so = WS_OX + (b * NH + h) * 7168 + 6144 + ((2 * kq + nn) & 7) * 128 + (lane >> 3) * 16;
        const unsigned yo = WS_Y + ((b * 40 + j8 * 5) * NH + h) * 128;
        float wa, wb;
        auto weights = [&](u32x4 st) {
          const float mj = __uint_as_float(st[0]);
          float Mh = fmaxf(mj, dpp<XOR1>(mj));
          Mh = fmaxf(Mh, dpp<XOR2>(Mh));
          Mh = fmaxf(Mh, dpp<XOR7>(Mh));
          const float ej = __builtin_amdgcn_exp2f((mj - Mh) * LOG2E);
          float Lh = ej * __uint_as_float(st[2]);
          Lh += dpp<XOR1>(Lh); Lh += dpp<XOR2>(Lh); Lh += dpp<XOR7>(Lh);
          const float wj = ej / Lh;
          wa = dpp<0x150>(wj); wb = dpp<0x151>(wj);
        };
        {
          lds_wait<16>(CT + CT_ST, b + 1);
          lazy(LAZY);
          u32x4 st;
#pragma nounroll
          for (int it = 0; it < SPIN_MAX; ++it) {
            if (it == 0) st = bld<0>(wsr, so, 0); else st = bld<SC1>(wsr, so, 0);
            if (__builtin_amdgcn_ballot_w64(bad2(st, tlo) != 0) == 0) break;
            __builtin_amdgcn_s_sleep(48);
            asm volatile("" ::: "memory");
          }
          weights(st);
          asm volatile("" : "+v"(wa), "+v"(wb));
        }
        lds_wait<1>(CT + CT_PO, b + 1);
        if (b < 3) lazy(LAZY);
        else { __builtin_amdgcn_s_sleep(P_W0); wait_until<1>(__builtin_amdgcn_readfirstlane(CT[CT_TL]) + P_X); }
        float t[5];
#pragma nounroll
        for (int it = 0; it < SPIN_MAX; ++it) {
          u32x4 z[6];
          if (it == 0 && b < 3) {
#pragma unroll
            for (int i = 0; i < 6; ++i) z[i] = bld<0>(wsr, oo, i * 1024);
          } else {
#pragma unroll
            for (int i = 0; i < 6; ++i) z[i] = bld<SC1>(wsr, oo, i * 1024);
          }
          SCHED();
          f32x4 D[10];
          unsigned bad = 0;
#pragma unroll
          for (int c = 0; c < 10; ++c) D[c] = f32x4{0.f, 0.f, 0.f, 0.f};
#pragma unroll
          for (int kb = 0; kb < 3; ++kb) {
            const u32x4 a = {z[2 * kb][0], z[2 * kb][2], z[2 * kb + 1][0], z[2 * kb + 1][2]};
            bad |= bad2(z[2 * kb], tlo) | bad2(z[2 * kb + 1], tlo);
#pragma unroll
            for (int c = 0; c < 5; ++c) { D[c] = mfma(a, F[kb][c], D[c]); D[5 + c] = mfma(a, F[kb + 3][c], D[5 + c]); }
          }
#pragma unroll
          for (int c = 0; c < 5; ++c) t[c] = wa * (D[c][0] + D[5 + c][1]) + wb * (D[c][2] + D[5 + c][3]);
          if (__builtin_amdgcn_ballot_w64(bad != 0) == 0) {
            break;
          }
          __builtin_amdgcn_s_sleep(P_W1);
          asm volatile("" ::: "memory");
        }
        float y0 = rs16(rs32(t[0], t[2]), rs32(t[1], t[3]));
        y0 = __int_as_float(__builtin_amdgcn_ds_bpermute(((lane & 3) * 16 + (lane >> 2)) * 4, __float_as_int(y0)));
        y0 += m0;
        t[4] = rs32(t[4], t[4]);
        const float y1 = rs16(t[4], t[4]) + m1;
        if (b < 3) {
          float* yd = (float*)(lds + L_YM) + b * 80;
          yd[lane] = y0;
          yd[64 + nn] = y1;
        } else {
          pub2(wsr, yo + kq * (NH * 128) + nn * 8, u32x2{__float_as_uint(y0), tlo});
          pub2(wsr, lane < 16 ? yo + 4 * (NH * 128) + nn * 8 : OOB, u32x2{__float_as_uint(y1), tlo});
        }
        lds_post(CT + CT_PY, lane);
      }
    } else if (hw == 2) {
      const rsrc_t w2r = mkrs(w2 + (size_t)h * 48 * (DM * 2) + j8 * 160, 47 * (DM * 2) + 160);
      u32x2 w[12], bb[3];
      unsigned u[12];
#pragma unroll
      for (int j = 0; j < 12; ++j) {
        wait_until<8>(tb + MLP_T0 + j * MLP_DT);
        LOAD80(w[j], u[j], w2r, kq * (12 * DM * 2) + nn * 8, kq * (12 * DM * 2) + 128 + nn * 2, j * (DM * 2));
        SCHED();
      }
#pragma unroll
      for (int j = 0; j < 3; ++j) bb[j] = *(const u32x2*)(b1 + h * 48 + kq * 12 + j * 4);
      const unsigned ho = WS_QX + (((32 + (h >> 2)) * 4 + (nn >> 2)) * 2 * DH + (h & 3) * 48 + kq * 12) * 8;
      u32x4 z[12];
#pragma nounroll
      for (int it = 0; it < SPIN_MAX; ++it) {
        unsigned bad = 0;
        if (it == 0) {
#pragma unroll
          for (int i = 0; i < 12; ++i) z[i] = bld<0>(wsr, ho + (i % 6) * 16 + (i / 6) * (DH * 8), 0);
        } else {
#pragma unroll
          for (int i = 0; i < 12; ++i) z[i] = bld<SC1>(wsr, ho + (i % 6) * 16 + (i / 6) * (DH * 8), 0);
        }
#pragma unroll
        for (int i = 0; i < 12; ++i) bad |= bad2(z[i], tlo);
        if (__builtin_amdgcn_ballot_w64(bad != 0) == 0) break;
        __builtin_amdgcn_s_sleep(48);
        asm volatile("" ::: "memory");
      }
      f32x4 D[5];
#pragma unroll
      for (int c = 0; c < 5; ++c) D[c] = f32x4{0.f, 0.f, 0.f, 0.f};
#pragma unroll
      for (int j = 0; j < 3; ++j) {
        u32x2 a;
#pragma unroll
        for (int k = 0; k < 2; ++k) {
          const u32x4 p = z[2 * j + k], q = z[6 + 2 * j + k];
          float h0 = rbf(rbf(__uint_as_float(p[0]) + __uint_as_float(q[0])) + bflo(bb[j][k]));
          float h1 = rbf(rbf(__uint_as_float(p[2]) + __uint_as_float(q[2])) + bfhi(bb[j][k]));
          h0 = h0 > 0.f ? h0 : 0.f; h1 = h1 > 0.f ? h1 : 0.f;
          a[k] = pkbf(h0 * h0, h1 * h1);
        }
        const u32x2* v = w + 4 * j;
        D[0] = mfma16(a, u32x2{lolo(v[0][0], v[1][0]), lolo(v[2][0], v[3][0])}, D[0]);
        D[1] = mfma16(a, u32x2{hihi(v[0][0], v[1][0]), hihi(v[2][0], v[3][0])}, D[1]);
        D[2] = mfma16(a, u32x2{lolo(v[0][1], v[1][1]), lolo(v[2][1], v[3][1])}, D[2]);
        D[3] = mfma16(a, u32x2{hihi(v[0][1], v[1][1]), hihi(v[2][1], v[3][1])}, D[3]);
        D[4] = mfma16(a, u32x2{lolo(u[4 * j], u[4 * j + 1]), lolo(u[4 * j + 2], u[4 * j + 3])}, D[4]);
      }
      float* ym = (float*)(lds + L_YM) + kq * 80;
      *(f32x4*)(ym + 4 * nn) = f32x4{D[0][0], D[1][0], D[2][0], D[3][0]};
      ym[64 + nn] = D[4][0];
      lds_post(CT + CT_YM, lane);
      {
        lds_wait<16>(CT + CT_PY, 3);
        f32x2 yv[3];
#pragma unroll
        for (int b = 0; b < 3; ++b) yv[b] = *((const f32x2*)(lds + L_YM) + b * 40 + (lane < 40 ? lane : 0));
        const unsigned yo = lane < 40 ? WS_Y + ((j8 * 5 + (lane >> 3)) * NH + h) * 128 + (lane & 7) * 16 : OOB;
        lds_wait<8>(CT + CT_TL, 1);
        wait_until<1>(__builtin_amdgcn_readfirstlane(CT[CT_TL]) + Y_L);
#pragma unroll
        for (int b = 0; b < 3; ++b) pub4(wsr, yo + b * (40 * NH * 128), u32x4{__float_as_uint(yv[b][0]), tlo, __float_as_uint(yv[b][1]), tlo});
      }
    } else {
      if (bid < 160) {
      const int rb = bid / 40, col = (bid % 40) * 16 + (lane & 7) * 2;
      const unsigned xv = *(const unsigned*)(x + rb * DM + col), bv = *(const unsigned*)(b2 + col);
      const float r0 = bflo(xv) + bflo(bv), r1 = bfhi(xv) + bfhi(bv);
      lds_wait<1>(CT + CT_PY, rb + 1);
      if (rb < 3) { lds_wait<8>(CT + CT_TL, 1); wait_until<2>(__builtin_amdgcn_readfirstlane(CT[CT_TG]) + R_L); }
      else { __builtin_amdgcn_s_sleep(R_W0); wait_until<1>(__builtin_amdgcn_readfirstlane(CT[CT_TG]) + R_X); }
      u32x4 z[4];
#pragma nounroll
      for (int it = 0; it < SPIN_MAX; ++it) {
#pragma unroll
        for (int i = 0; i < 4; ++i) z[i] = bld<SC1>(wsr, WS_Y + bid * (NH * 128) + lane * 16, i * 1024);
        if (__builtin_amdgcn_ballot_w64((bad2(z[0], tlo) | bad2(z[1], tlo) | bad2(z[2], tlo) | bad2(z[3], tlo)) != 0) == 0) {
          break;
        }
        __builtin_amdgcn_s_sleep(R_W1);
        asm volatile("" ::: "memory");
      }
      float s0 = (__uint_as_float(z[0][0]) + __uint_as_float(z[1][0])) + (__uint_as_float(z[2][0]) + __uint_as_float(z[3][0]));
      float s1 = (__uint_as_float(z[0][2]) + __uint_as_float(z[1][2])) + (__uint_as_float(z[2][2]) + __uint_as_float(z[3][2]));
      s0 += dpp<ROR8>(s0); s1 += dpp<ROR8>(s1);
      s0 = rs16(s0, s0); s1 = rs16(s1, s1);
      s0 = rs32(s0, s0); s1 = rs32(s1, s1);
      __builtin_amdgcn_raw_buffer_store_b32(pkbf(r0 + s0, r1 + s1), mkrs(out, NB * DM * 2), lane < 8 ? (rb * DM + col) * 2 : OOB, 0, SC1);
      if (bid == 0) pub2(wsr, lane == 0 ? WS_EP : OOB, u32x2{tlo, thi});
      }
    }
  } else {
    const int sw = wave - 4, g = lane >> 3, c = lane & 7;
    const size_t base = (size_t)h * SK * (DH * 2);
    constexpr size_t BSTR = (size_t)NH * SK * DH * 2;
    const unsigned loff = g * (DH * 2) + c * 16;
    int tc = sw * GW + j8 + h * RA;
    const unsigned po = (((bid >> 3) & 15) == 0 && sw == 0 && lane < 4) ? WS_PC + 4096 + ((bid & 7) * 2 + (bid >> 7)) * 256 + lane * 16 : OOB;
    const unsigned ro = lane < 16 ? WS_PC + lane * 256 : OOB;
    int nt = sw * (TAU16 / 8) + (j8 * 4 + (h & 3)) * (TAU16 / 256), dmin = -100000, vo = T0OFF * 16, sg = vo, dv = 0, tcu = TAUC16 / 2, n = 2;
    u32x4 R[16][3], W[3][3];
#define ISSUE(j, r, CM) do { vo += dv; const int d_ = max(vo + nt, dmin); const unsigned o_ = (unsigned)(tc & TMASK) * TILE_B; \
      const int l_ = wait_until<1>(tb + (unsigned)(d_ >> 4)), f_ = loads_in_flight(); SCHED(); \
      CM; \
      _Pragma("unroll") for (int i_ = 0; i_ < 3; ++i_) R[j][i_] = bld<NT>(r, loff + i_ * 128, o_); SCHED(); \
      const int z_ = d_ + l_ * 16 - nt + f_ * (TAU16 / 6); sg = (sg * 3 + z_) >> 2; nt += TAU16 / 2; dmin = d_ + l_ * 16 + tcu; tc += TSTEP; SCHED(); } while (0)
#define KTILE(j, J, pk) do { \
      float s0_ = 0.f, s1_ = 0.f, s2_ = 0.f; \
      _Pragma("unroll") for (int k_ = 0; k_ < 4; ++k_) { \
        s0_ = dot2(R[j][0][k_], q[0][k_], s0_); s1_ = dot2(R[j][1][k_], q[1][k_], s1_); s2_ = dot2(R[j][2][k_], q[2][k_], s2_); } \
      float s_ = (s0_ + s1_) + s2_; \
      s_ += dpp<XOR1>(s_); s_ += dpp<XOR2>(s_); s_ += dpp<XOR7>(s_); \
      pk = c == (J) ? s_ : pk; \
      asm volatile("" : "+v"(pk) :: "memory"); SCHED(); } while (0)
#define VTILE(j) do { \
      const float p_ = bcast8<j>(S0); \
      _Pragma("unroll") for (int i_ = 0; i_ < 3; ++i_) _Pragma("unroll") for (int k_ = 0; k_ < 4; ++k_) \
        acc[i_ * 4 + k_] = __builtin_elementwise_fma(f32x2{bflo(R[j][i_][k_]), bfhi(R[j][i_][k_])}, f32x2{p_, p_}, acc[i_ * 4 + k_]); \
      asm volatile("" : "+v"(acc[0]), "+v"(acc[1]), "+v"(acc[2]), "+v"(acc[3]), "+v"(acc[4]), "+v"(acc[5]), \
                        "+v"(acc[6]), "+v"(acc[7]), "+v"(acc[8]), "+v"(acc[9]), "+v"(acc[10]), "+v"(acc[11]) :: "memory"); SCHED(); } while (0)
#define CONTROL(j) do { \
      if ((j) == C_AT) { \
        const int d_ = (int)(e - tb * 16u) - vo; \
        int y_ = (d_ > -32000 && d_ < 32000) ? d_ : (n > 2 ? C_MISS : 0); \
        y_ = (y_ + dppi<XOR1>(y_)) >> 1; \
        y_ = max(y_, dppi<XOR2>(y_)); y_ = max(y_, dppi<XOR7>(y_)); y_ = max(y_, dppi<ROR8>(y_)); \
        const int u_ = __builtin_amdgcn_readfirstlane(y_), x_ = max(-C_NEG, min(u_ - C_SLACK, C_CAP)); \
        dv = (x_ * C_G + max(0, x_ - C_K) * C_G2) >> 11; SCHED(); } } while (0)
#define CTLMEM(j) do { \
      if ((j) == 0) e = __builtin_amdgcn_raw_buffer_load_b32(wsr, ro, n * 4096, 0); \
      if ((j) == C_WR) { const unsigned v_ = tb * 16u + (unsigned)sg; __builtin_amdgcn_raw_buffer_store_b128(u32x4{v_, v_, v_, v_}, wsr, po, n * 4096, SC1); } } while (0)
    {
      const rsrc_t krs = mkrs(kc + base, PART_B);
#pragma unroll
      for (int j = 0; j < 16; ++j) {
        if (j == PF3) {
#pragma unroll
          for (int p = 0; p < 3; ++p) {
            const int u = p * 32 + sw * 8 + j8;
            const rsrc_t pr = mkrs(u < 80 ? wo + (size_t)h * DH * (DM * 2) + u * TILE_B : w2 + (size_t)h * 48 * (DM * 2) + (u - 80) * TILE_B, TILE_B);
            wait_until<1>(tb + (unsigned)((vo + nt) >> 4));
            SCHED();
#pragma unroll
            for (int i = 0; i < 3; ++i) W[p][i] = bld<0>(pr, lane * 16, i * 1024);
            SCHED();
            nt += TAU16 / 2;
          }
        }
        if (j < PRE) vo -= T0OFF * 16;
        ISSUE((j + 8) & 15, krs, (void)0);
        if (j < PRE) vo += T0OFF * 16;
      }
      asm volatile("" :: "v"(W[0][0]), "v"(W[0][1]), "v"(W[0][2]), "v"(W[1][0]), "v"(W[1][1]), "v"(W[1][2]), "v"(W[2][0]), "v"(W[2][1]), "v"(W[2][2]));
    }
    BARRIER();
    u32x4 q[3];
#pragma unroll
    for (int i = 0; i < 3; ++i) q[i] = *(const u32x4*)(lds + L_QB + 128 * i + 16 * c);
    float S0 = 0.f, S1 = 0.f, S2 = 0.f, S3 = 0.f;
    {
      const rsrc_t krs = mkrs(kc + base, PART_B);
#pragma unroll
      for (int j = 0; j < 8; ++j) { KTILE(j, j, S3); ISSUE(j, krs, (void)0); KTILE(8 + j, j, S2); }
    }
    int it = 2;
#pragma nounroll
    for (int b = 0; b < NB; ++b) {
#pragma nounroll
      for (; it < 4; ++it, ++n) {
        const rsrc_t r = mkrs((it < 3 ? kc : vc) + base + (size_t)b * BSTR, PART_B);
        unsigned e;
        S0 = S1; S1 = S2; S2 = S3;
#pragma unroll
        for (int j = 0; j < 8; ++j) { KTILE(j, j, S3); CONTROL(j); ISSUE(j, r, CTLMEM(j)); }
      }
      {
        S0 = rbf(rbf(S0) * SCALE); S1 = rbf(rbf(S1) * SCALE); S2 = rbf(rbf(S2) * SCALE); S3 = rbf(rbf(S3) * SCALE);
        float m = fmaxf(fmaxf(S0, S1), fmaxf(S2, S3));
        m = fmaxf(m, dpp<XOR1>(m)); m = fmaxf(m, dpp<XOR2>(m)); m = fmaxf(m, dpp<XOR7>(m));
        m = max_x16_32(fmaxf(m, dpp<ROR8>(m)));
        S0 = __builtin_amdgcn_exp2f((S0 - m) * LOG2E); S1 = __builtin_amdgcn_exp2f((S1 - m) * LOG2E);
        S2 = __builtin_amdgcn_exp2f((S2 - m) * LOG2E); S3 = __builtin_amdgcn_exp2f((S3 - m) * LOG2E);
        const float l = wave_sum((S0 + S1) + (S2 + S3));
        if (lane == 0) *(f32x2*)(lds + L_ML + b * 32 + sw * 8) = f32x2{m, l};
        lds_post(CT + CT_ML, lane);
      }
      f32x2 acc[12];
#pragma unroll
      for (int k = 0; k < 12; ++k) acc[k] = f32x2{0.f, 0.f};
#pragma nounroll
      for (it = 0; it < 4; ++it, ++n) {
        const rsrc_t r = mkrs(it < 3 ? vc + base + (size_t)b * BSTR : kc + base + (size_t)((b + 1) & 3) * BSTR, n == 31 ? 0 : PART_B);
        if (n == 31) { if (sw == 3) *(u32x2*)(CT + CT_TL) = u32x2{(tb + ((vo + nt) >> 4)) | 1, tb + ((vo + nt) >> 4)}; nt -= 1 << 24; dmin = nt; tcu = -(1 << 24); }
        unsigned e;
        VTILE(0); CONTROL(0); ISSUE(0, r, CTLMEM(0)); VTILE(1); CONTROL(1); ISSUE(1, r, CTLMEM(1));
        VTILE(2); CONTROL(2); ISSUE(2, r, CTLMEM(2)); VTILE(3); CONTROL(3); ISSUE(3, r, CTLMEM(3));
        VTILE(4); CONTROL(4); ISSUE(4, r, CTLMEM(4)); VTILE(5); CONTROL(5); ISSUE(5, r, CTLMEM(5));
        VTILE(6); CONTROL(6); ISSUE(6, r, CTLMEM(6)); VTILE(7); CONTROL(7); ISSUE(7, r, CTLMEM(7));
        S0 = S1; S1 = S2; S2 = S3;
      }
      float* const OA = (float*)(lds + L_OA + b * 3072) + sw * 192 + 8 * c + g;
#pragma unroll
      for (int i = 0; i < 3; ++i) {
        float u[4], v[2];
#pragma unroll
        for (int k = 0; k < 4; ++k) u[k] = rs32(acc[i * 4 + (k >> 1)][k & 1], acc[i * 4 + 2 + (k >> 1)][k & 1]);
#pragma unroll
        for (int k = 0; k < 2; ++k) v[k] = rs16(u[k], u[k + 2]);
        OA[64 * i] = rs8(v[0], v[1], lane & 8);
      }
      lds_post(CT + CT_OA + b, lane);
      if (b < 3) {
        lds_wait<1>(CT + CT_Q, 3);
#pragma unroll
        for (int i = 0; i < 3; ++i) q[i] = *(const u32x4*)(lds + L_QB + (b + 1) * 384 + 128 * i + 16 * c);
      }
      it = 0;
    }
  }
}

extern "C" void kernel_launch(void* const* d_in, const int* in_sizes, int n_in, void* d_out, int out_size, void* d_ws,
                              size_t ws_size, hipStream_t stream) {
  (void)in_sizes; (void)n_in; (void)out_size; (void)ws_size;
  fused_block<<<NWG, NTHR, 0, stream>>>((const unsigned short*)d_in[0], (const unsigned char*)d_in[1],
                                        (const unsigned char*)d_in[2], (const unsigned char*)d_in[3],
                                        (const unsigned char*)d_in[4], (const unsigned char*)d_in[6],
                                        (const unsigned short*)d_in[7], (const unsigned char*)d_in[8],
                                        (const unsigned short*)d_in[9], (unsigned char*)d_ws, (unsigned short*)d_out);
}
```
